```python
import torch

def _softmax(x, axis=-1):
    return torch.softmax(x, dim=axis)
def _gelu(x):
    return torch.nn.functional.gelu(x, approximate="tanh")
def _silu(x):
    return torch.nn.functional.silu(x)
def _sigmoid(x):
    return torch.sigmoid(x)
def _layernorm(x, axis=-1, eps=1e-5):
    m = x.mean(dim=axis, keepdim=True)
    v = x.var(dim=axis, keepdim=True, unbiased=False)
    return (x - m) / torch.sqrt(v + eps)
def _scatter_add(tgt, idx, src, axis=0):
    return tgt.clone().index_add_(axis, idx, src)
def _segment_sum(src, seg, n):
    out = torch.zeros((n,) + src.shape[1:], dtype=src.dtype)
    return out.index_add_(0, seg, src)
def _pad(x, pw):
    flat = tuple(v for pair in reversed(pw) for v in pair)
    return torch.nn.functional.pad(x, flat)

kernel_name = 'concat_heads__sliding_block__sliding_block__prompt'

def setup_inputs():

    _w = (lambda a: (lambda t: t.to(torch.bfloat16) if t.is_floating_point() else t)(torch.as_tensor(a)))
    def _A_setup():
        B, S, D = 1, 2048, 576
        return {
            "x": _w((lambda *s: torch.randn(*s, dtype=torch.bfloat16) * 0.2)(B, S, D)),
            "wqkv": _w((lambda *s: torch.randn(*s, dtype=torch.bfloat16) * 0.2)(D, 3 * D)),
            "wo": _w((lambda *s: torch.randn(*s, dtype=torch.bfloat16) * 0.05)(D, D)),
        }
    _A_in = _A_setup()

    def _B_setup():
        B, S, D = 1, 2048, 576
        return {
            "x": _w((lambda *s: torch.randn(*s, dtype=torch.bfloat16) * 0.2)(B, S, D)),
            "wqkv": _w((lambda *s: torch.randn(*s, dtype=torch.bfloat16) * 0.2)(D, 3 * D)),
            "wo": _w((lambda *s: torch.randn(*s, dtype=torch.bfloat16) * 0.05)(D, D)),
        }
    _B_in = _B_setup()

    return {
        "A_x": _A_in["x"],
        "A_wqkv": _A_in["wqkv"],
        "A_wo": _A_in["wo"],
        "B_x": _B_in["x"],
        "B_wqkv": _B_in["wqkv"],
        "B_wo": _B_in["wo"],
    }

def reference(A_x, A_wqkv, A_wo, B_x, B_wqkv, B_wo):
    def _A_ref(x, wqkv, wo):
        B, S, D = x.shape
        H = 32; Dh = D // H; W = 256
        qkv = (lambda x, *d: x.permute(*d))((x @ wqkv).reshape(B, S, 3, H, Dh), 2, 0, 3, 1, 4)
        q, k, v = qkv[0], qkv[1], qkv[2]
        scores = torch.einsum("bhqd,bhkd->bhqk", q, k) * (Dh ** -0.5)
        idx = torch.arange(S)
        mask = (idx[:, None] - idx[None, :] >= 0) & (idx[:, None] - idx[None, :] < W)
        scores = torch.where(mask[None, None], scores, -1e9)
        p = _softmax(scores, axis=-1)
        o = torch.einsum("bhqk,bhkd->bhqd", p, v)
        return (lambda x, *d: x.permute(*d))(o, 0, 2, 1, 3).reshape(B, S, D) @ wo

    def _B_ref(x, wqkv, wo):
        B, S, D = x.shape
        H = 32; Dh = D // H; W = 256
        qkv = (lambda x, *d: x.permute(*d))((x @ wqkv).reshape(B, S, 3, H, Dh), 2, 0, 3, 1, 4)
        q, k, v = qkv[0], qkv[1], qkv[2]
        scores = torch.einsum("bhqd,bhkd->bhqk", q, k) * (Dh ** -0.5)
        idx = torch.arange(S)
        mask = (idx[:, None] - idx[None, :] >= 0) & (idx[:, None] - idx[None, :] < W)
        scores = torch.where(mask[None, None], scores, -1e9)
        p = _softmax(scores, axis=-1)
        o = torch.einsum("bhqk,bhkd->bhqd", p, v)
        return (lambda x, *d: x.permute(*d))(o, 0, 2, 1, 3).reshape(B, S, D) @ wo

    a = _A_ref(A_x, A_wqkv, A_wo)
    b = _B_ref(B_x, B_wqkv, B_wo)
    return torch.concatenate([a, b], axis=-1)
```

```cpp
#include <hip/hip_runtime.h>
#include <hip/hip_bf16.h>
typedef unsigned short u16;
__device__ __forceinline__ float b2f(u16 v){ return __uint_as_float(((unsigned)v)<<16); }
__device__ __forceinline__ u16 f2b(float f){ __hip_bfloat16 b = __float2bfloat16(f); return *(u16*)&b; }

__global__ void gemm_naive(const u16* A, const u16* B, u16* C, int M, int N, int K, int ldc) {
    __shared__ float As[16][17], Bs[16][17];
    int tx = threadIdx.x & 15, ty = threadIdx.x >> 4;
    int row = blockIdx.y * 16 + ty, col = blockIdx.x * 16 + tx;
    float acc = 0.f;
    for (int k0 = 0; k0 < K; k0 += 16) {
        As[ty][tx] = b2f(A[row * K + k0 + tx]);
        Bs[ty][tx] = b2f(B[(k0 + ty) * N + col]);
        __syncthreads();
        for (int k = 0; k < 16; ++k) acc += As[ty][k] * Bs[k][tx];
        __syncthreads();
    }
    C[row * ldc + col] = f2b(acc);
}
__global__ void attn_naive(const u16* qkv, u16* o) {
    int h = blockIdx.y, i = blockIdx.x * 64 + threadIdx.x;
    float q[18];
    #pragma unroll
    for (int d = 0; d < 18; ++d) q[d] = b2f(qkv[i * 1728 + h * 18 + d]);
    float m = -1e30f, l = 0.f, acc[18];
    #pragma unroll
    for (int d = 0; d < 18; ++d) acc[d] = 0.f;
    int j0 = i - 255 < 0 ? 0 : i - 255;
    for (int j = j0; j <= i; ++j) {
        float s = 0.f;
        #pragma unroll
        for (int d = 0; d < 18; ++d) s += q[d] * b2f(qkv[j * 1728 + 576 + h * 18 + d]);
        s *= 0.23570226039551584f;
        float mn = fmaxf(m, s), a = __expf(m - mn), p = __expf(s - mn);
        l = l * a + p;
        #pragma unroll
        for (int d = 0; d < 18; ++d) acc[d] = acc[d] * a + p * b2f(qkv[j * 1728 + 1152 + h * 18 + d]);
        m = mn;
    }
    #pragma unroll
    for (int d = 0; d < 18; ++d) o[i * 576 + h * 18 + d] = f2b(acc[d] / l);
}
extern "C" void kernel_launch(void* const* d_in, const int* in_sizes, int n_in,
                              void* d_out, int out_size, void* d_ws, size_t ws_size, hipStream_t stream) {
    u16* ws = (u16*)d_ws;
    for (int b = 0; b < 2; ++b) {
        const u16* x = (const u16*)d_in[3 * b], *w = (const u16*)d_in[3 * b + 1], *wo = (const u16*)d_in[3 * b + 2];
        u16* qkv = ws + b * (2048 * 1728 + 2048 * 576); u16* o = qkv + 2048 * 1728;
        gemm_naive<<<dim3(1728 / 16, 2048 / 16), 256, 0, stream>>>(x, w, qkv, 2048, 1728, 576, 1728);
        attn_naive<<<dim3(2048 / 64, 32), 64, 0, stream>>>(qkv, o);
        gemm_naive<<<dim3(576 / 16, 2048 / 16), 256, 0, stream>>>(o, wo, (u16*)d_out + b * 576, 2048, 576, 576, 1152);
    }
}
```

```cpp
#include <hip/hip_runtime.h>

__global__ __launch_bounds__(512, 2) void fused_attn_kernel(const void* xA, const void* wA, const void* woA, void* outA,
                                                            const void* xB, const void* wB, const void* woB, void* outB,
                                                            void* ws) {
    __shared__ __attribute__((aligned(1024))) char lds[163840];
    asm volatile(
        "s_bfe_u32 s3, s2, 0x10002\n"
        "s_lshl_b32 s4, s3, 5\n"
        "s_load_dwordx8 s[8:15], s[0:1], s4\n"
        "s_load_dwordx2 s[16:17], s[0:1], 0x40\n"
        "v_lshrrev_b32 v1, 6, v0\n"
        "v_lshlrev_b32 v2, 2, v0\n"
        "v_readfirstlane_b32 s5, v1\n"
        "v_mov_b32 v4, 0\n"
        "v_add_u32 v2, 159744, v2\n"
        "ds_write_b32 v2, v4\n"
        "v_and_b32 v0, 63, v0\n"
        "s_mov_b32 s56, 0x400000\n"
        "s_and_b32 s18, s2, 1\n"
        "s_lshl_b32 s18, s18, 3\n"
        "s_lshr_b32 s57, s2, 5\n"
        "s_add_u32 s18, s18, s57\n"
        "s_bfe_u32 s19, s2, 0x10001\n"
        "s_lshl_b32 s19, s19, 2\n"
        "s_bfe_u32 s58, s2, 0x20003\n"
        "s_add_u32 s19, s19, s58\n"
        "s_and_b32 s6, s5, 3\n"
        "s_cmp_ge_u32 s5, 4\n"
        "s_cbranch_scc0 L_LOADER\n"
        "s_getpc_b64 s[60:61]\n"
        "s_lshr_b32 s4, s2, 3\n"
        "s_lshl_b32 s4, s4, 2\n"
        "s_add_u32 s4, s4, s6\n"
        "s_lshl_b32 s4, s4, 7\n"
        "v_and_b32 v1, 3, v0\n"
        "v_lshl_add_u32 v1, v1, 14, s4\n"
        "v_min_u32 v1, L_END-L_LOADER, v1\n"
        "global_load_dword v3, v1, s[60:61]\n"
        "s_branch L_COMPUTE\n"
        "L_LOADER:\n"
        "s_mul_i32 s58, s58, 2\n"
        "s_mul_i32 s57, s57, 1\n"
        "s_add_u32 s57, s57, s58\n"
        "s_mul_i32 s58, s57, 57\n"
        "s_lshr_b32 s58, s58, 9\n"
        "s_mul_i32 s58, s58, 9\n"
        "s_sub_u32 s57, s57, s58\n"
        "s_lshl_b32 s68, s57, 7\n"
        "s_mul_i32 s69, s57, 221184\n"
        "s_add_u32 s69, s69, 1152\n"
        "s_lshl_b32 s70, s6, 12\n"
        "s_mul_i32 s71, s6, 3072\n"
        "s_mul_i32 s4, s6, 36864\n"
        "s_mul_i32 s7, s6, 192\n"
        "v_lshrrev_b32 v1, 3, v0\n"
        "v_lshrrev_b32 v2, 4, v0\n"
        "v_mul_u32_u24 v1, 1152, v1\n"
        "v_xor_b32 v2, v2, v0\n"
        "v_and_b32 v2, 7, v2\n"
        "v_lshlrev_b32 v2, 4, v2\n"
        "v_add3_u32 v8, v1, v2, s4\n"
        "v_xor_b32 v1, 64, v8\n"
        "v_add_u32 v9, 8192, v1\n"
        "v_add_u32 v10, 16384, v8\n"
        "v_add_u32 v11, 24576, v1\n"
        "v_add_u32 v1, s7, v0\n"
        "v_mul_u32_u24 v2, 7282, v1\n"
        "v_lshrrev_b32 v2, 16, v2\n"
        "v_mad_i32_i24 v3, v2, -9, v1\n"
        "v_and_b32 v4, 1, v2\n"
        "v_bfe_u32 v5, v2, 1, 2\n"
        "v_and_b32 v2, 0x38, v2\n"
        "v_lshl_or_b32 v4, v4, 2, v5\n"
        "v_or_b32 v2, v2, v4\n"
        "v_mul_u32_u24 v2, 3456, v2\n"
        "v_lshl_add_u32 v12, v3, 4, v2\n"
        "v_add_u32 v1, 64, v1\n"
        "v_mul_u32_u24 v2, 7282, v1\n"
        "v_lshrrev_b32 v2, 16, v2\n"
        "v_mad_i32_i24 v3, v2, -9, v1\n"
        "v_and_b32 v4, 1, v2\n"
        "v_bfe_u32 v5, v2, 1, 2\n"
        "v_and_b32 v2, 0x38, v2\n"
        "v_lshl_or_b32 v4, v4, 2, v5\n"
        "v_or_b32 v2, v2, v4\n"
        "v_mul_u32_u24 v2, 3456, v2\n"
        "v_lshl_add_u32 v13, v3, 4, v2\n"
        "v_subrev_u32 v13, 1024, v13\n"
        "v_add_u32 v1, 64, v1\n"
        "v_mul_u32_u24 v2, 7282, v1\n"
        "v_lshrrev_b32 v2, 16, v2\n"
        "v_mad_i32_i24 v3, v2, -9, v1\n"
        "v_and_b32 v4, 1, v2\n"
        "v_bfe_u32 v5, v2, 1, 2\n"
        "v_and_b32 v2, 0x38, v2\n"
        "v_lshl_or_b32 v4, v4, 2, v5\n"
        "v_or_b32 v2, v2, v4\n"
        "v_mul_u32_u24 v2, 3456, v2\n"
        "v_lshl_add_u32 v14, v3, 4, v2\n"
        "v_subrev_u32 v14, 2048, v14\n"
        "s_mul_i32 s57, s18, 147456\n"
        "s_mul_i32 s58, s19, 144\n"
        "s_sub_u32 s30, 1990656, s58\n"
        "s_mov_b32 s26, 147456\n"
        "s_mov_b32 s27, 0x20000\n"
        "s_mov_b32 s31, 0x20000\n"
        "s_waitcnt lgkmcnt(0)\n"
        "s_add_u32 s24, s8, s57\n"
        "s_addc_u32 s25, s9, 0\n"
        "s_add_u32 s28, s10, s58\n"
        "s_addc_u32 s29, s11, 0\n"
        "s_and_b32 s25, s25, 0xffff\n"
        "s_and_b32 s29, s29, 0xffff\n"
        "s_add_u32 m0, s70, 49152\n"
        "s_nop 0\n"
        "buffer_load_dwordx4 v8, s[24:27], s68 offen offset:0 lds\n"
        "buffer_load_dwordx4 v9, s[24:27], s68 offen offset:1024 lds\n"
        "buffer_load_dwordx4 v10, s[24:27], s68 offen offset:2048 lds\n"
        "buffer_load_dwordx4 v11, s[24:27], s68 offen offset:3072 lds\n"
        "s_cmp_eq_u32 s6, 3\n"
        "s_cbranch_scc1 L_LD0\n"
        "s_add_u32 m0, s71, 150528\n"
        "s_nop 0\n"
        "buffer_load_dwordx4 v12, s[28:31], s69 offen offset:0 lds\n"
        "buffer_load_dwordx4 v13, s[28:31], s69 offen offset:1024 lds\n"
        "buffer_load_dwordx4 v14, s[28:31], s69 offen offset:2048 lds\n"
        "L_LD0:\n"
        "s_load_dwordx2 s[22:23], s[16:17], 0x0\n"
        "s_setprio 3\n"
        "s_lshl_b32 s20, s3, 3\n"
        "s_add_u32 s20, s20, s19\n"
        "s_lshl_b32 s20, s20, 4\n"
        "s_add_u32 s20, s20, s18\n"
        "s_and_b32 s83, s20, 127\n"
        "s_lshl_b32 s83, s83, 17\n"
        "s_lshl_b32 s21, s18, 2\n"
        "s_add_u32 s21, s21, s6\n"
        "s_lshl_b32 s21, s21, 17\n"
        "s_lshr_b32 s4, s83, 21\n"
        "s_add_u32 s4, s4, s3\n"
        "s_add_u32 s4, s4, 0\n"
        "s_and_b32 s4, s4, 1\n"
        "s_lshl_b32 s4, s4, 13\n"
        "s_add_u32 s83, s83, s4\n"
        "s_add_u32 s83, s83, 4194304\n"
        "s_lshr_b32 s4, s21, 21\n"
        "s_add_u32 s4, s4, s3\n"
        "s_add_u32 s4, s4, 0\n"
        "s_and_b32 s4, s4, 1\n"
        "s_lshl_b32 s4, s4, 13\n"
        "s_add_u32 s21, s21, s4\n"
        "s_add_u32 s21, s21, 20971520\n"
        "s_mov_b32 s72, 1990656\n"
        "s_mov_b32 s73, 221184\n"
        "v_and_b32 v1, 3, v0\n"
        "v_mul_u32_u24 v1, 260, v1\n"
        "v_add_u32 v16, 159744, v1\n"
        "v_add_u32 v17, 160768, v1\n"
        "s_lshl_b32 s4, s6, 8\n"
        "s_add_u32 s4, s4, 160768\n"
        "v_and_b32 v1, 31, v0\n"
        "v_lshl_add_u32 v18, v1, 2, s4\n"
        "v_lshlrev_b32 v19, 4, v0\n"
        "s_add_u32 s40, s16, 0\n"
        "s_addc_u32 s41, s17, 0\n"
        "s_and_b32 s41, s41, 0xffff\n"
        "s_mov_b32 s42, 30408704\n"
        "s_mov_b32 s43, 0x20000\n"
        "s_sub_u32 s57, s83, 262144\n"
        "s_sub_u32 s58, s83, 131072\n"
        "s_add_u32 s32, s16, s57\n"
        "s_addc_u32 s33, s17, 0\n"
        "s_and_b32 s33, s33, 0xffff\n"
        "s_mov_b32 s34, 73728\n"
        "s_mov_b32 s35, 0x20000\n"
        "s_add_u32 s36, s16, s58\n"
        "s_addc_u32 s37, s17, 0\n"
        "s_and_b32 s37, s37, 0xffff\n"
        "s_mov_b32 s38, 73728\n"
        "s_mov_b32 s39, 0x20000\n"
        "s_cmp_ge_u32 s18, 2\n"
        "s_cselect_b32 s34, 73728, 0\n"
        "s_cmp_ge_u32 s18, 1\n"
        "s_cselect_b32 s38, 73728, 0\n"
        "s_add_u32 s79, s83, 67584\n"
        "v_mul_u32_u24 v1, 131080, v0\n"
        "v_add_u32 v1, -8, v1\n"
        "v_cmp_eq_u32 vcc, 0, v0\n"
        "s_sub_u32 s58, 15, s18\n"
        "s_min_u32 s58, s58, 2\n"
        "v_cndmask_b32 v1, v1, 16, vcc\n"
        "v_cmp_ge_u32 vcc, s58, v0\n"
        "v_bfrev_b32 v2, 2\n"
        "s_nop 0\n"
        "v_cndmask_b32 v24, v2, v1, vcc\n"
        "s_cmp_eq_u32 s6, 1\n"
        "s_cselect_b64 s[36:37], s[32:33], s[36:37]\n"
        "s_cselect_b32 s38, s34, s38\n"
        "s_cselect_b32 s74, 0, 8192\n"
        "s_and_b32 s57, s6, 1\n"
        "s_lshl_b32 s57, s57, 1\n"
        "s_lshr_b32 s58, s6, 1\n"
        "s_mul_i32 s58, s58, 36\n"
        "s_add_u32 s57, s57, s58\n"
        "s_add_u32 s57, s57, 16\n"
        "s_mul_i32 s78, s57, 1152\n"
        "s_sub_u32 s44, s12, 2048\n"
        "s_subb_u32 s45, s13, 0\n"
        "s_and_b32 s45, s45, 0xffff\n"
        "s_mov_b32 s46, 665600\n"
        "s_mov_b32 s47, 0x20000\n"
        "s_mul_i32 s76, s6, 165888\n"
        "s_mul_i32 s77, s6, 6144\n"
        "s_add_u32 s48, s16, s21\n"
        "s_addc_u32 s49, s17, 0\n"
        "s_and_b32 s49, s49, 0xffff\n"
        "s_mov_b32 s50, 73728\n"
        "s_mov_b32 s51, 0x20000\n"
        "s_lshl_b32 s57, s18, 2\n"
        "s_add_u32 s57, s57, s6\n"
        "s_mul_i32 s57, s57, 73728\n"
        "s_add_u32 s52, s14, s57\n"
        "s_addc_u32 s53, s15, 0\n"
        "s_and_b32 s53, s53, 0xffff\n"
        "s_mov_b32 s54, 72576\n"
        "s_mov_b32 s55, 0x20000\n"
        "s_lshl_b32 s59, s19, 7\n"
        "s_add_u32 s4, s59, 2048\n"
        "s_lshr_b32 s7, s19, 2\n"
        "s_lshl_b32 s7, s7, 6\n"
        "s_add_u32 s7, s7, 1024\n"
        "s_movk_i32 s58, 1152\n"
        "v_bfe_u32 v1, v0, 4, 1\n"
        "v_lshlrev_b32 v2, 2, v1\n"
        "v_xor_b32 v2, v2, v0\n"
        "v_and_b32 v2, 7, v2\n"
        "v_lshl_add_u32 v2, v2, 4, s4\n"
        "v_and_b32 v3, 3, v0\n"
        "v_lshl_add_u32 v3, v3, 4, s7\n"
        "v_lshrrev_b32 v4, 3, v0\n"
        "v_mad_u32_u24 v26, v4, s58, v2\n"
        "v_add_u32 v27, 8192, v26\n"
        "v_lshrrev_b32 v4, 2, v0\n"
        "v_mad_u32_u24 v28, v4, s58, v3\n"
        "v_lshrrev_b32 v4, 5, v0\n"
        "v_lshl_add_u32 v5, v4, 2, v1\n"
        "v_mul_u32_u24 v5, 72, v5\n"
        "v_bfe_u32 v6, v0, 3, 1\n"
        "v_add_u32 v5, v5, v6\n"
        "v_mad_u32_u24 v29, v5, s58, v2\n"
        "v_add_u32 v30, 164864, v29\n"
        "v_lshl_add_u32 v5, v1, 1, v4\n"
        "v_lshl_add_u32 v5, v5, 1, v6\n"
        "v_mul_u32_u24 v5, 72, v5\n"
        "v_bfe_u32 v7, v0, 2, 1\n"
        "v_add_u32 v5, v5, v7\n"
        "v_mad_u32_u24 v31, v5, s58, v3\n"
        "v_and_b32 v2, 7, v0\n"
        "v_and_b32 v3, 31, v0\n"
        "v_lshlrev_b32 v5, 9, v4\n"
        "v_lshl_add_u32 v15, v3, 2, v5\n"
        "v_bfe_u32 v5, v0, 2, 2\n"
        "v_lshl_add_u32 v5, v4, 2, v5\n"
        "v_and_b32 v7, 1, v0\n"
        "v_lshl_add_u32 v1, v1, 1, v7\n"
        "v_bfe_u32 v7, v0, 1, 1\n"
        "v_lshlrev_b32 v7, 3, v7\n"
        "v_lshl_add_u32 v1, v1, 4, v7\n"
        "v_lshl_add_u32 v7, v5, 6, v1\n"
        "v_add_u32 v34, 2048, v7\n"
        "v_lshl_add_u32 v1, v6, 6, v1\n"
        "v_lshl_add_u32 v32, v5, 7, v1\n"
        "v_xor_b32 v33, 64, v32\n"
        "v_add_u32 v35, 122880, v32\n"
        "v_add_u32 v38, 49152, v32\n"
        "v_add_u32 v36, 122880, v33\n"
        "v_add_u32 v39, 49152, v33\n"
        "v_add_u32 v37, 122880, v34\n"
        "v_add_u32 v40, 49152, v34\n"
        "s_lshl_b32 s4, s77, 1\n"
        "s_add_u32 s4, s4, 73728\n"
        "v_add_u32 v25, s4, v15\n"
        "s_lshl_b32 s58, s6, 8\n"
        "s_add_u32 s58, s58, 159744\n"
        "v_mov_b32 v53, s58\n"
        "s_movk_i32 s58, 2304\n"
        "v_xor_b32 v1, v2, v4\n"
        "v_lshlrev_b32 v1, 4, v1\n"
        "v_lshl_add_u32 v1, v3, 7, v1\n"
        "v_add_u32 v41, s4, v1\n"
        "v_xor_b32 v42, 32, v41\n"
        "v_xor_b32 v43, 64, v41\n"
        "v_xor_b32 v44, 0x60, v41\n"
        "v_bfe_u32 v1, v0, 1, 2\n"
        "v_xor_b32 v1, v1, v4\n"
        "v_lshlrev_b32 v1, 4, v1\n"
        "v_lshl_add_u32 v1, v3, 6, v1\n"
        "v_add_u32 v45, s4, v1\n"
        "v_xor_b32 v46, 32, v45\n"
        "v_add_u32 v47, s4, v19\n"
        "v_lshrrev_b32 v1, 3, v0\n"
        "v_xor_b32 v2, v1, v0\n"
        "v_and_b32 v3, 7, v2\n"
        "v_lshl_add_u32 v3, v3, 4, s59\n"
        "v_mad_u32_u24 v48, v1, s58, v3\n"
        "v_and_b32 v3, 3, v2\n"
        "v_lshl_add_u32 v3, v3, 4, s7\n"
        "v_lshrrev_b32 v1, 2, v0\n"
        "v_mad_u32_u24 v49, v1, s58, v3\n"
        "v_add_u32 v196, 16384, v19\n"
        "v_add_u32 v197, 65536, v19\n"
        "v_add_u32 v198, 114688, v19\n"
        "v_lshlrev_b32 v1, 3, v0\n"
        "v_lshrrev_b32 v2, 4, v0\n"
        "v_lshl_or_b32 v205, v2, 28, v1\n"
        "v_and_b32 v1, 31, v0\n"
        "v_lshlrev_b32 v1, 2, v1\n"
        "v_add_u32 v202, 161664, v1\n"
        "v_add_u32 v204, 161408, v1\n"
        "s_and_b32 s75, s19, 3\n"
        "s_xor_b32 s75, s75, 0\n"
        "s_lshr_b32 s4, s2, 4\n"
        "s_mul_i32 s4, s4, 41472\n"
        "s_add_u32 s4, s4, 2048\n"
        "s_add_u32 s7, s19, 1\n"
        "s_bitcmp1_b32 s7, 1\n"
        "s_cselect_b32 s4, s4, 0x10000000\n"
        "s_and_b32 s7, s19, 4\n"
        "v_add_u32 v1, 0, v0\n"
        "v_min_u32 v1, 179, v1\n"
        "v_mul_u32_u24 v2, 13108, v1\n"
        "v_lshrrev_b32 v2, 16, v2\n"
        "v_mad_i32_i24 v1, v2, -5, v1\n"
        "v_cmp_ne_u32 vcc, 4, v1\n"
        "v_add_u32 v1, s7, v1\n"
        "s_nop 0\n"
        "v_cndmask_b32 v1, 8, v1, vcc\n"
        "v_mul_u32_u24 v2, 1152, v2\n"
        "v_lshlrev_b32 v1, 7, v1\n"
        "v_add3_u32 v50, v1, v2, s4\n"
        "v_add_u32 v1, 64, v0\n"
        "v_min_u32 v1, 179, v1\n"
        "v_mul_u32_u24 v2, 13108, v1\n"
        "v_lshrrev_b32 v2, 16, v2\n"
        "v_mad_i32_i24 v1, v2, -5, v1\n"
        "v_cmp_ne_u32 vcc, 4, v1\n"
        "v_add_u32 v1, s7, v1\n"
        "s_nop 0\n"
        "v_cndmask_b32 v1, 8, v1, vcc\n"
        "v_mul_u32_u24 v2, 1152, v2\n"
        "v_lshlrev_b32 v1, 7, v1\n"
        "v_add3_u32 v51, v1, v2, s4\n"
        "v_add_u32 v1, 128, v0\n"
        "v_min_u32 v1, 179, v1\n"
        "v_mul_u32_u24 v2, 13108, v1\n"
        "v_lshrrev_b32 v2, 16, v2\n"
        "v_mad_i32_i24 v1, v2, -5, v1\n"
        "v_cmp_ne_u32 vcc, 4, v1\n"
        "v_add_u32 v1, s7, v1\n"
        "s_nop 0\n"
        "v_cndmask_b32 v1, 8, v1, vcc\n"
        "v_mul_u32_u24 v2, 1152, v2\n"
        "v_lshlrev_b32 v1, 7, v1\n"
        "v_add3_u32 v52, v1, v2, s4\n"
        "s_cmp_eq_u32 s6, 3\n"
        "s_cbranch_scc1 L_XWAVE\n"
        "s_add_u32 s14, s69, 1152\n"
        "s_add_u32 s68, s68, 128\n"
        "s_cmp_eq_u32 s68, 1152\n"
        "s_cselect_b32 s68, 0, s68\n"
        "s_add_u32 s69, s69, s73\n"
        "s_cmp_ge_u32 s69, s72\n"
        "s_cselect_b32 s7, s72, 0\n"
        "s_sub_u32 s69, s69, s7\n"
        "s_add_u32 m0, s70, 73728\n"
        "s_nop 0\n"
        "buffer_load_dwordx4 v8, s[24:27], s68 offen offset:0 lds\n"
        "buffer_load_dwordx4 v9, s[24:27], s68 offen offset:1024 lds\n"
        "buffer_load_dwordx4 v10, s[24:27], s68 offen offset:2048 lds\n"
        "buffer_load_dwordx4 v11, s[24:27], s68 offen offset:3072 lds\n"
        "s_add_u32 m0, s71, 141312\n"
        "s_nop 0\n"
        "buffer_load_dwordx4 v12, s[28:31], s69 offen offset:0 lds\n"
        "buffer_load_dwordx4 v13, s[28:31], s69 offen offset:1024 lds\n"
        "buffer_load_dwordx4 v14, s[28:31], s69 offen offset:2048 lds\n"
        "s_add_u32 s68, s68, 128\n"
        "s_cmp_eq_u32 s68, 1152\n"
        "s_cselect_b32 s68, 0, s68\n"
        "s_add_u32 s69, s69, s73\n"
        "s_cmp_ge_u32 s69, s72\n"
        "s_cselect_b32 s7, s72, 0\n"
        "s_sub_u32 s69, s69, s7\n"
        "s_waitcnt vmcnt(7) lgkmcnt(0)\n"
        "s_barrier\n"
        "s_add_u32 m0, s70, 24576\n"
        "s_nop 0\n"
        "buffer_load_dwordx4 v8, s[24:27], s68 offen offset:0 lds\n"
        "buffer_load_dwordx4 v9, s[24:27], s68 offen offset:1024 lds\n"
        "buffer_load_dwordx4 v10, s[24:27], s68 offen offset:2048 lds\n"
        "buffer_load_dwordx4 v11, s[24:27], s68 offen offset:3072 lds\n"
        "s_add_u32 m0, s71, 132096\n"
        "s_nop 0\n"
        "buffer_load_dwordx4 v12, s[28:31], s69 offen offset:0 lds\n"
        "buffer_load_dwordx4 v13, s[28:31], s69 offen offset:1024 lds\n"
        "buffer_load_dwordx4 v14, s[28:31], s69 offen offset:2048 lds\n"
        "s_add_u32 s68, s68, 128\n"
        "s_cmp_eq_u32 s68, 1152\n"
        "s_cselect_b32 s68, 0, s68\n"
        "s_add_u32 s69, s69, s73\n"
        "s_cmp_ge_u32 s69, s72\n"
        "s_cselect_b32 s7, s72, 0\n"
        "s_sub_u32 s69, s69, s7\n"
        "s_waitcnt vmcnt(7)\n"
        "s_barrier\n"
        "s_add_u32 s22, s22, 0x7f4a7c18\n"
        "s_addc_u32 s23, s23, 0x9e3779b9\n"
        "s_and_b32 s22, s22, -16\n"
        "s_or_b64 s[62:63], s[22:23], 15\n"
        "v_mov_b32 v207, s23\n"
        "s_add_u32 m0, s70, 49152\n"
        "s_nop 0\n"
        "buffer_load_dwordx4 v8, s[24:27], s68 offen offset:0 lds\n"
        "buffer_load_dwordx4 v9, s[24:27], s68 offen offset:1024 lds\n"
        "buffer_load_dwordx4 v10, s[24:27], s68 offen offset:2048 lds\n"
        "buffer_load_dwordx4 v11, s[24:27], s68 offen offset:3072 lds\n"
        "s_add_u32 m0, s71, 122880\n"
        "s_nop 0\n"
        "buffer_load_dwordx4 v12, s[28:31], s69 offen offset:0 lds\n"
        "buffer_load_dwordx4 v13, s[28:31], s69 offen offset:1024 lds\n"
        "buffer_load_dwordx4 v14, s[28:31], s69 offen offset:2048 lds\n"
        "s_add_u32 s68, s68, 128\n"
        "s_cmp_eq_u32 s68, 1152\n"
        "s_cselect_b32 s68, 0, s68\n"
        "s_add_u32 s69, s69, s73\n"
        "s_cmp_ge_u32 s69, s72\n"
        "s_cselect_b32 s7, s72, 0\n"
        "s_sub_u32 s69, s69, s7\n"
        "s_waitcnt vmcnt(7)\n"
        "s_barrier\n"
        "s_add_u32 m0, s70, 73728\n"
        "s_nop 0\n"
        "buffer_load_dwordx4 v8, s[24:27], s68 offen offset:0 lds\n"
        "buffer_load_dwordx4 v9, s[24:27], s68 offen offset:1024 lds\n"
        "buffer_load_dwordx4 v10, s[24:27], s68 offen offset:2048 lds\n"
        "buffer_load_dwordx4 v11, s[24:27], s68 offen offset:3072 lds\n"
        "s_add_u32 m0, s71, 0\n"
        "s_nop 0\n"
        "buffer_load_dwordx4 v12, s[28:31], s69 offen offset:0 lds\n"
        "buffer_load_dwordx4 v13, s[28:31], s69 offen offset:1024 lds\n"
        "buffer_load_dwordx4 v14, s[28:31], s69 offen offset:2048 lds\n"
        "s_add_u32 s68, s68, 128\n"
        "s_cmp_eq_u32 s68, 1152\n"
        "s_cselect_b32 s68, 0, s68\n"
        "s_add_u32 s69, s69, s73\n"
        "s_cmp_ge_u32 s69, s72\n"
        "s_cselect_b32 s7, s72, 0\n"
        "s_sub_u32 s69, s69, s7\n"
        "s_waitcnt vmcnt(7)\n"
        "s_barrier\n"
        "s_add_u32 m0, s71, 150528\n"
        "s_nop 0\n"
        "buffer_load_dwordx4 v12, s[28:31], s14 offen offset:0 lds\n"
        "buffer_load_dwordx4 v13, s[28:31], s14 offen offset:1024 lds\n"
        "buffer_load_dwordx4 v14, s[28:31], s14 offen offset:2048 lds\n"
        "s_add_u32 s14, s14, s73\n"
        "s_cmp_ge_u32 s14, s72\n"
        "s_cselect_b32 s7, s72, 0\n"
        "s_sub_u32 s14, s14, s7\n"
        "s_waitcnt vmcnt(3)\n"
        "s_barrier\n"
        "s_add_u32 m0, s70, 49152\n"
        "s_nop 0\n"
        "buffer_load_dwordx4 v8, s[24:27], s68 offen offset:0 lds\n"
        "buffer_load_dwordx4 v9, s[24:27], s68 offen offset:1024 lds\n"
        "buffer_load_dwordx4 v10, s[24:27], s68 offen offset:2048 lds\n"
        "buffer_load_dwordx4 v11, s[24:27], s68 offen offset:3072 lds\n"
        "s_add_u32 m0, s71, 141312\n"
        "s_nop 0\n"
        "buffer_load_dwordx4 v12, s[28:31], s69 offen offset:0 lds\n"
        "buffer_load_dwordx4 v13, s[28:31], s69 offen offset:1024 lds\n"
        "buffer_load_dwordx4 v14, s[28:31], s69 offen offset:2048 lds\n"
        "s_add_u32 s68, s68, 128\n"
        "s_cmp_eq_u32 s68, 1152\n"
        "s_cselect_b32 s68, 0, s68\n"
        "s_add_u32 s69, s69, s73\n"
        "s_cmp_ge_u32 s69, s72\n"
        "s_cselect_b32 s7, s72, 0\n"
        "s_sub_u32 s69, s69, s7\n"
        "s_waitcnt vmcnt(7)\n"
        "s_barrier\n"
        "s_add_u32 m0, s71, 132096\n"
        "s_nop 0\n"
        "buffer_load_dwordx4 v12, s[28:31], s14 offen offset:0 lds\n"
        "buffer_load_dwordx4 v13, s[28:31], s14 offen offset:1024 lds\n"
        "buffer_load_dwordx4 v14, s[28:31], s14 offen offset:2048 lds\n"
        "s_add_u32 s14, s14, s73\n"
        "s_cmp_ge_u32 s14, s72\n"
        "s_cselect_b32 s7, s72, 0\n"
        "s_sub_u32 s14, s14, s7\n"
        "s_add_u32 m0, s70, 73728\n"
        "s_nop 0\n"
        "buffer_load_dwordx4 v8, s[24:27], s68 offen offset:0 lds\n"
        "buffer_load_dwordx4 v9, s[24:27], s68 offen offset:1024 lds\n"
        "buffer_load_dwordx4 v10, s[24:27], s68 offen offset:2048 lds\n"
        "buffer_load_dwordx4 v11, s[24:27], s68 offen offset:3072 lds\n"
        "s_add_u32 m0, s71, 122880\n"
        "s_nop 0\n"
        "buffer_load_dwordx4 v12, s[28:31], s69 offen offset:0 lds\n"
        "buffer_load_dwordx4 v13, s[28:31], s69 offen offset:1024 lds\n"
        "buffer_load_dwordx4 v14, s[28:31], s69 offen offset:2048 lds\n"
        "s_add_u32 s68, s68, 128\n"
        "s_cmp_eq_u32 s68, 1152\n"
        "s_cselect_b32 s68, 0, s68\n"
        "s_add_u32 s69, s69, s73\n"
        "s_cmp_ge_u32 s69, s72\n"
        "s_cselect_b32 s7, s72, 0\n"
        "s_sub_u32 s69, s69, s7\n"
        "s_waitcnt vmcnt(10)\n"
        "s_barrier\n"
        "s_waitcnt vmcnt(7)\n"
        "s_barrier\n"
        "s_add_u32 m0, s71, 0\n"
        "s_nop 0\n"
        "buffer_load_dwordx4 v12, s[28:31], s14 offen offset:0 lds\n"
        "buffer_load_dwordx4 v13, s[28:31], s14 offen offset:1024 lds\n"
        "buffer_load_dwordx4 v14, s[28:31], s14 offen offset:2048 lds\n"
        "s_add_u32 s14, s14, s73\n"
        "s_cmp_ge_u32 s14, s72\n"
        "s_cselect_b32 s7, s72, 0\n"
        "s_sub_u32 s14, s14, s7\n"
        "s_add_u32 m0, s70, 49152\n"
        "s_nop 0\n"
        "buffer_load_dwordx4 v8, s[24:27], s68 offen offset:0 lds\n"
        "buffer_load_dwordx4 v9, s[24:27], s68 offen offset:1024 lds\n"
        "buffer_load_dwordx4 v10, s[24:27], s68 offen offset:2048 lds\n"
        "buffer_load_dwordx4 v11, s[24:27], s68 offen offset:3072 lds\n"
        "s_add_u32 m0, s71, 150528\n"
        "s_nop 0\n"
        "buffer_load_dwordx4 v12, s[28:31], s69 offen offset:0 lds\n"
        "buffer_load_dwordx4 v13, s[28:31], s69 offen offset:1024 lds\n"
        "buffer_load_dwordx4 v14, s[28:31], s69 offen offset:2048 lds\n"
        "s_add_u32 s68, s68, 128\n"
        "s_cmp_eq_u32 s68, 1152\n"
        "s_cselect_b32 s68, 0, s68\n"
        "s_add_u32 s69, s69, s73\n"
        "s_cmp_ge_u32 s69, s72\n"
        "s_cselect_b32 s7, s72, 0\n"
        "s_sub_u32 s69, s69, s7\n"
        "s_waitcnt vmcnt(10)\n"
        "s_barrier\n"
        "s_load_dwordx2 s[8:9], s[16:17], 128\n"
        "s_waitcnt vmcnt(7)\n"
        "s_barrier\n"
        "s_add_u32 m0, s71, 141312\n"
        "s_nop 0\n"
        "buffer_load_dwordx4 v12, s[28:31], s14 offen offset:0 lds\n"
        "buffer_load_dwordx4 v13, s[28:31], s14 offen offset:1024 lds\n"
        "buffer_load_dwordx4 v14, s[28:31], s14 offen offset:2048 lds\n"
        "s_add_u32 s14, s14, s73\n"
        "s_cmp_ge_u32 s14, s72\n"
        "s_cselect_b32 s7, s72, 0\n"
        "s_sub_u32 s14, s14, s7\n"
        "s_add_u32 m0, s70, 73728\n"
        "s_nop 0\n"
        "buffer_load_dwordx4 v8, s[24:27], s68 offen offset:0 lds\n"
        "buffer_load_dwordx4 v9, s[24:27], s68 offen offset:1024 lds\n"
        "buffer_load_dwordx4 v10, s[24:27], s68 offen offset:2048 lds\n"
        "buffer_load_dwordx4 v11, s[24:27], s68 offen offset:3072 lds\n"
        "s_add_u32 m0, s71, 132096\n"
        "s_nop 0\n"
        "buffer_load_dwordx4 v12, s[28:31], s69 offen offset:0 lds\n"
        "buffer_load_dwordx4 v13, s[28:31], s69 offen offset:1024 lds\n"
        "buffer_load_dwordx4 v14, s[28:31], s69 offen offset:2048 lds\n"
        "s_add_u32 s69, s69, 220032\n"
        "s_cmp_ge_u32 s69, s72\n"
        "s_cselect_b32 s7, s72, 0\n"
        "s_sub_u32 s69, s69, s7\n"
        "s_waitcnt lgkmcnt(0)\n"
        "Lpw_1:\n"
        "s_xor_b64 s[10:11], s[8:9], s[22:23]\n"
        "s_or_b32 s10, s10, 2\n"
        "s_cmp_eq_u64 s[10:11], 3\n"
        "s_cbranch_scc0 Lpws_2\n"
        "s_and_b32 s57, s8, 2\n"
        "s_lshl_b32 s57, s57, 12\n"
        "s_add_u32 s40, s40, s57\n"
        "s_addc_u32 s41, s41, 0\n"
        "s_add_u32 s32, s32, s57\n"
        "s_addc_u32 s33, s33, 0\n"
        "s_add_u32 s36, s36, s57\n"
        "s_addc_u32 s37, s37, 0\n"
        "s_add_u32 s48, s48, s57\n"
        "s_addc_u32 s49, s49, 0\n"
        "s_add_u32 s57, s21, s59\n"
        "s_add_u32 s57, s57, 69632\n"
        "s_load_dword s12, s[40:41], s57 glc\n"
        "s_waitcnt vmcnt(10)\n"
        "s_barrier\n"
        "s_waitcnt vmcnt(7)\n"
        "s_barrier\n"
        "s_add_u32 m0, s71, 122880\n"
        "s_nop 0\n"
        "buffer_load_dwordx4 v12, s[28:31], s14 offen offset:0 lds\n"
        "buffer_load_dwordx4 v13, s[28:31], s14 offen offset:1024 lds\n"
        "buffer_load_dwordx4 v14, s[28:31], s14 offen offset:2048 lds\n"
        "s_add_u32 s14, s14, s73\n"
        "s_cmp_ge_u32 s14, s72\n"
        "s_cselect_b32 s7, s72, 0\n"
        "s_sub_u32 s14, s14, s7\n"
        "s_add_u32 m0, s71, 49152\n"
        "s_nop 0\n"
        "buffer_load_dwordx4 v12, s[28:31], s14 offen offset:0 lds\n"
        "buffer_load_dwordx4 v13, s[28:31], s14 offen offset:1024 lds\n"
        "buffer_load_dwordx4 v14, s[28:31], s14 offen offset:2048 lds\n"
        "s_add_u32 s14, s14, s73\n"
        "s_cmp_ge_u32 s14, s72\n"
        "s_cselect_b32 s7, s72, 0\n"
        "s_sub_u32 s14, s14, s7\n"
        "s_waitcnt lgkmcnt(0)\n"
        "s_waitcnt vmcnt(6)\n"
        "s_barrier\n"
        "s_add_u32 m0, s71, 0\n"
        "s_nop 0\n"
        "buffer_load_dwordx4 v12, s[28:31], s14 offen offset:0 lds\n"
        "buffer_load_dwordx4 v13, s[28:31], s14 offen offset:1024 lds\n"
        "buffer_load_dwordx4 v14, s[28:31], s14 offen offset:2048 lds\n"
        "s_add_u32 s14, s14, s73\n"
        "s_cmp_ge_u32 s14, s72\n"
        "s_cselect_b32 s7, s72, 0\n"
        "s_sub_u32 s14, s14, s7\n"
        "s_add_u32 m0, s71, 150528\n"
        "s_nop 0\n"
        "buffer_load_dwordx4 v12, s[28:31], s14 offen offset:0 lds\n"
        "buffer_load_dwordx4 v13, s[28:31], s14 offen offset:1024 lds\n"
        "buffer_load_dwordx4 v14, s[28:31], s14 offen offset:2048 lds\n"
        "s_add_u32 s14, s14, s73\n"
        "s_cmp_ge_u32 s14, s72\n"
        "s_cselect_b32 s7, s72, 0\n"
        "s_sub_u32 s14, s14, s7\n"
        "s_waitcnt vmcnt(9)\n"
        "s_barrier\n"
        "s_add_u32 m0, s71, 141312\n"
        "s_nop 0\n"
        "buffer_load_dwordx4 v12, s[28:31], s14 offen offset:0 lds\n"
        "buffer_load_dwordx4 v13, s[28:31], s14 offen offset:1024 lds\n"
        "buffer_load_dwordx4 v14, s[28:31], s14 offen offset:2048 lds\n"
        "s_add_u32 m0, s71, 73728\n"
        "s_nop 0\n"
        "buffer_load_dwordx4 v12, s[28:31], s69 offen offset:0 lds\n"
        "buffer_load_dwordx4 v13, s[28:31], s69 offen offset:1024 lds\n"
        "buffer_load_dwordx4 v14, s[28:31], s69 offen offset:2048 lds\n"
        "s_add_u32 s69, s69, s73\n"
        "s_cmp_ge_u32 s69, s72\n"
        "s_cselect_b32 s7, s72, 0\n"
        "s_sub_u32 s69, s69, s7\n"
        "s_waitcnt vmcnt(12)\n"
        "s_barrier\n"
        "s_add_u32 m0, s71, 132096\n"
        "s_nop 0\n"
        "buffer_load_dwordx4 v12, s[28:31], s69 offen offset:0 lds\n"
        "buffer_load_dwordx4 v13, s[28:31], s69 offen offset:1024 lds\n"
        "buffer_load_dwordx4 v14, s[28:31], s69 offen offset:2048 lds\n"
        "s_add_u32 s69, s69, s73\n"
        "s_cmp_ge_u32 s69, s72\n"
        "s_cselect_b32 s7, s72, 0\n"
        "s_sub_u32 s69, s69, s7\n"
        "s_waitcnt vmcnt(12)\n"
        "s_barrier\n"
        "s_add_u32 m0, s71, 122880\n"
        "s_nop 0\n"
        "buffer_load_dwordx4 v12, s[28:31], s69 offen offset:0 lds\n"
        "buffer_load_dwordx4 v13, s[28:31], s69 offen offset:1024 lds\n"
        "buffer_load_dwordx4 v14, s[28:31], s69 offen offset:2048 lds\n"
        "s_add_u32 s69, s69, s73\n"
        "s_cmp_ge_u32 s69, s72\n"
        "s_cselect_b32 s7, s72, 0\n"
        "s_sub_u32 s69, s69, s7\n"
        "s_waitcnt vmcnt(12)\n"
        "s_barrier\n"
        "s_add_u32 m0, s71, 49152\n"
        "s_nop 0\n"
        "buffer_load_dwordx4 v12, s[28:31], s69 offen offset:0 lds\n"
        "buffer_load_dwordx4 v13, s[28:31], s69 offen offset:1024 lds\n"
        "buffer_load_dwordx4 v14, s[28:31], s69 offen offset:2048 lds\n"
        "s_add_u32 s69, s69, s73\n"
        "s_cmp_ge_u32 s69, s72\n"
        "s_cselect_b32 s7, s72, 0\n"
        "s_sub_u32 s69, s69, s7\n"
        "s_waitcnt vmcnt(12)\n"
        "s_barrier\n"
        "s_waitcnt vmcnt(9)\n"
        "s_barrier\n"
        "s_add_u32 m0, s71, 150528\n"
        "s_nop 0\n"
        "buffer_load_dwordx4 v12, s[28:31], s69 offen offset:0 lds\n"
        "buffer_load_dwordx4 v13, s[28:31], s69 offen offset:1024 lds\n"
        "buffer_load_dwordx4 v14, s[28:31], s69 offen offset:2048 lds\n"
        "s_add_u32 s69, s69, s73\n"
        "s_cmp_ge_u32 s69, s72\n"
        "s_cselect_b32 s7, s72, 0\n"
        "s_sub_u32 s69, s69, s7\n"
        "s_waitcnt vmcnt(9)\n"
        "s_barrier\n"
        "s_add_u32 m0, s71, 141312\n"
        "s_nop 0\n"
        "buffer_load_dwordx4 v12, s[28:31], s69 offen offset:0 lds\n"
        "buffer_load_dwordx4 v13, s[28:31], s69 offen offset:1024 lds\n"
        "buffer_load_dwordx4 v14, s[28:31], s69 offen offset:2048 lds\n"
        "s_add_u32 s69, s69, s73\n"
        "s_cmp_ge_u32 s69, s72\n"
        "s_cselect_b32 s7, s72, 0\n"
        "s_sub_u32 s69, s69, s7\n"
        "s_waitcnt vmcnt(9)\n"
        "s_barrier\n"
        "s_add_u32 m0, s71, 73728\n"
        "s_nop 0\n"
        "buffer_load_dwordx4 v12, s[28:31], s69 offen offset:0 lds\n"
        "buffer_load_dwordx4 v13, s[28:31], s69 offen offset:1024 lds\n"
        "buffer_load_dwordx4 v14, s[28:31], s69 offen offset:2048 lds\n"
        "s_add_u32 s69, s69, s73\n"
        "s_cmp_ge_u32 s69, s72\n"
        "s_cselect_b32 s7, s72, 0\n"
        "s_sub_u32 s69, s69, s7\n"
        "s_waitcnt vmcnt(6)\n"
        "s_barrier\n"
        "s_add_u32 m0, s71, 132096\n"
        "s_nop 0\n"
        "buffer_load_dwordx4 v12, s[28:31], s69 offen offset:0 lds\n"
        "buffer_load_dwordx4 v13, s[28:31], s69 offen offset:1024 lds\n"
        "buffer_load_dwordx4 v14, s[28:31], s69 offen offset:2048 lds\n"
        "s_add_u32 s69, s69, s73\n"
        "s_cmp_ge_u32 s69, s72\n"
        "s_cselect_b32 s7, s72, 0\n"
        "s_sub_u32 s69, s69, s7\n"
        "s_add_u32 m0, s71, 24576\n"
        "s_nop 0\n"
        "buffer_load_dwordx4 v12, s[28:31], s69 offen offset:0 lds\n"
        "buffer_load_dwordx4 v13, s[28:31], s69 offen offset:1024 lds\n"
        "buffer_load_dwordx4 v14, s[28:31], s69 offen offset:2048 lds\n"
        "v_mov_b32 v22, 4\n"
        "s_waitcnt vmcnt(6)\n"
        "ds_write_b32 v18, v22\n"
        "s_cmp_eq_u32 s6, 2\n"
        "s_cbranch_scc1 L_WK2\n"
        "v_mov_b32 v22, 6\n"
        "s_waitcnt vmcnt(0)\n"
        "ds_write_b32 v18, v22\n"
        "s_cmp_eq_u32 s6, 2\n"
        "s_cbranch_scc1 L_WK2\n"
        "s_add_u32 s66, s40, s79\n"
        "s_addc_u32 s67, s41, 0\n"
        "s_cmp_eq_u32 s18, 0\n"
        "s_cbranch_scc1 Lnopoll_3\n"
        "s_branch Lspin_5\n"
        "Lsp_4:\n"
        "s_sleep 1\n"
        "s_sub_u32 s56, s56, 1\n"
        "s_cbranch_scc1 L_ABORT\n"
        "s_and_b32 s64, s56, 1023\n"
        "s_cbranch_scc0 Lvl_7\n"
        "Lspin_5:\n"
        "s_load_dwordx4 s[8:11], s[66:67], 0 glc\n"
        "Lspm_6:\n"
        "s_waitcnt lgkmcnt(0)\n"
        "s_cmp_eq_u32 s18, 1\n"
        "s_cselect_b64 s[10:11], s[8:9], s[10:11]\n"
        "s_and_b64 s[60:61], s[8:9], s[10:11]\n"
        "s_or_b64 s[64:65], s[8:9], s[10:11]\n"
        "s_or_b64 s[60:61], s[60:61], 14\n"
        "s_cmp_eq_u64 s[60:61], s[62:63]\n"
        "s_cbranch_scc0 Lsp_4\n"
        "s_or_b64 s[64:65], s[64:65], 15\n"
        "s_cmp_eq_u64 s[64:65], s[62:63]\n"
        "s_cbranch_scc0 Lsp_4\n"
        "Lnopoll_3:\n"
        "s_add_u32 m0, s74, 0\n"
        "s_mov_b32 s4, 0\n"
        "buffer_load_dwordx4 v19, s[36:39], s4 offen offset:0 sc1 lds\n"
        "buffer_load_dwordx4 v19, s[36:39], s4 offen offset:1024 sc1 lds\n"
        "buffer_load_dwordx4 v19, s[36:39], s4 offen offset:2048 sc1 lds\n"
        "buffer_load_dwordx4 v19, s[36:39], s4 offen offset:3072 sc1 lds\n"
        "s_add_u32 m0, s74, 4096\n"
        "s_mov_b32 s4, 4096\n"
        "buffer_load_dwordx4 v19, s[36:39], s4 offen offset:0 sc1 lds\n"
        "buffer_load_dwordx4 v19, s[36:39], s4 offen offset:1024 sc1 lds\n"
        "buffer_load_dwordx4 v19, s[36:39], s4 offen offset:2048 sc1 lds\n"
        "buffer_load_dwordx4 v19, s[36:39], s4 offen offset:3072 sc1 lds\n"
        "v_mov_b32 v22, 7\n"
        "s_waitcnt vmcnt(0)\n"
        "ds_write_b32 v18, v22\n"
        "s_setprio 0\n"
        "Lw_8:\n"
        "ds_read_b32 v20, v16\n"
        "s_waitcnt lgkmcnt(0)\n"
        "v_cmp_gt_u32 vcc, 27, v20\n"
        "s_cbranch_vccz Lwok_9\n"
        "s_sleep 1\n"
        "s_sub_u32 s56, s56, 1\n"
        "s_cbranch_scc1 L_ABORT\n"
        "s_branch Lw_8\n"
        "Lwok_9:\n"
        "s_cmp_eq_u32 s18, 0\n"
        "s_cbranch_scc1 Lnopoll_10\n"
        "s_branch Lspin_12\n"
        "Lsp_11:\n"
        "s_sleep 1\n"
        "s_sub_u32 s56, s56, 1\n"
        "s_cbranch_scc1 L_ABORT\n"
        "s_and_b32 s64, s56, 1023\n"
        "s_cbranch_scc0 Lvl_14\n"
        "Lspin_12:\n"
        "s_load_dwordx4 s[8:11], s[66:67], 0 glc\n"
        "Lspm_13:\n"
        "s_waitcnt lgkmcnt(0)\n"
        "s_cmp_eq_u32 s18, 1\n"
        "s_cselect_b64 s[10:11], s[8:9], s[10:11]\n"
        "s_and_b64 s[60:61], s[8:9], s[10:11]\n"
        "s_or_b64 s[64:65], s[8:9], s[10:11]\n"
        "s_or_b64 s[60:61], s[60:61], 13\n"
        "s_cmp_eq_u64 s[60:61], s[62:63]\n"
        "s_cbranch_scc0 Lsp_11\n"
        "s_or_b64 s[64:65], s[64:65], 15\n"
        "s_cmp_eq_u64 s[64:65], s[62:63]\n"
        "s_cbranch_scc0 Lsp_11\n"
        "Lnopoll_10:\n"
        "s_add_u32 m0, s74, 24576\n"
        "s_mov_b32 s4, 16384\n"
        "buffer_load_dwordx4 v19, s[36:39], s4 offen offset:0 sc1 lds\n"
        "buffer_load_dwordx4 v19, s[36:39], s4 offen offset:1024 sc1 lds\n"
        "buffer_load_dwordx4 v19, s[36:39], s4 offen offset:2048 sc1 lds\n"
        "buffer_load_dwordx4 v19, s[36:39], s4 offen offset:3072 sc1 lds\n"
        "s_add_u32 m0, s74, 28672\n"
        "s_mov_b32 s4, 20480\n"
        "buffer_load_dwordx4 v19, s[36:39], s4 offen offset:0 sc1 lds\n"
        "buffer_load_dwordx4 v19, s[36:39], s4 offen offset:1024 sc1 lds\n"
        "buffer_load_dwordx4 v19, s[36:39], s4 offen offset:2048 sc1 lds\n"
        "buffer_load_dwordx4 v19, s[36:39], s4 offen offset:3072 sc1 lds\n"
        "v_mov_b32 v22, 8\n"
        "s_waitcnt vmcnt(0)\n"
        "ds_write_b32 v18, v22\n"
        "s_add_u32 m0, s77, 122880\n"
        "s_add_u32 s4, s76, 0\n"
        "s_cmp_lg_u32 s75, 0\n"
        "buffer_load_dwordx4 v26, s[44:47], s4 offen offset:0 lds\n"
        "buffer_load_dwordx4 v27, s[44:47], s4 offen offset:1024 lds\n"
        "s_cbranch_scc1 Lno3_15\n"
        "buffer_load_dwordx4 v28, s[44:47], s4 offen offset:2048 lds\n"
        "Lno3_15:\n"
        "s_add_u32 m0, s77, 125952\n"
        "s_add_u32 s4, s76, 82944\n"
        "s_cmp_lg_u32 s75, 0\n"
        "buffer_load_dwordx4 v26, s[44:47], s4 offen offset:0 lds\n"
        "buffer_load_dwordx4 v27, s[44:47], s4 offen offset:1024 lds\n"
        "s_cbranch_scc1 Lno3_16\n"
        "buffer_load_dwordx4 v28, s[44:47], s4 offen offset:2048 lds\n"
        "Lno3_16:\n"
        "s_add_u32 m0, s71, 147456\n"
        "s_cmp_lg_u32 s75, 0\n"
        "buffer_load_dwordx4 v29, s[44:47], s78 offen offset:0 lds\n"
        "buffer_load_dwordx4 v30, s[44:47], s78 offen offset:1024 lds\n"
        "s_cbranch_scc1 Lno3_17\n"
        "buffer_load_dwordx4 v31, s[44:47], s78 offen offset:2048 lds\n"
        "Lno3_17:\n"
        "s_cmp_eq_u32 s18, 0\n"
        "s_cbranch_scc1 Lnopoll_18\n"
        "s_branch Lspin_20\n"
        "Lsp_19:\n"
        "s_sleep 1\n"
        "s_sub_u32 s56, s56, 1\n"
        "s_cbranch_scc1 L_ABORT\n"
        "s_and_b32 s64, s56, 1023\n"
        "s_cbranch_scc0 Lvl_22\n"
        "Lspin_20:\n"
        "s_load_dwordx4 s[8:11], s[66:67], 0 glc\n"
        "Lspm_21:\n"
        "s_waitcnt lgkmcnt(0)\n"
        "s_cmp_eq_u32 s18, 1\n"
        "s_cselect_b64 s[10:11], s[8:9], s[10:11]\n"
        "s_and_b64 s[60:61], s[8:9], s[10:11]\n"
        "s_or_b64 s[64:65], s[8:9], s[10:11]\n"
        "s_or_b64 s[60:61], s[60:61], 11\n"
        "s_cmp_eq_u64 s[60:61], s[62:63]\n"
        "s_cbranch_scc0 Lsp_19\n"
        "s_or_b64 s[64:65], s[64:65], 15\n"
        "s_cmp_eq_u64 s[64:65], s[62:63]\n"
        "s_cbranch_scc0 Lsp_19\n"
        "Lnopoll_18:\n"
        "s_add_u32 m0, s74, 49152\n"
        "s_mov_b32 s4, 32768\n"
        "buffer_load_dwordx4 v19, s[36:39], s4 offen offset:0 sc1 lds\n"
        "buffer_load_dwordx4 v19, s[36:39], s4 offen offset:1024 sc1 lds\n"
        "buffer_load_dwordx4 v19, s[36:39], s4 offen offset:2048 sc1 lds\n"
        "buffer_load_dwordx4 v19, s[36:39], s4 offen offset:3072 sc1 lds\n"
        "s_add_u32 m0, s74, 53248\n"
        "s_mov_b32 s4, 36864\n"
        "buffer_load_dwordx4 v19, s[36:39], s4 offen offset:0 sc1 lds\n"
        "buffer_load_dwordx4 v19, s[36:39], s4 offen offset:1024 sc1 lds\n"
        "buffer_load_dwordx4 v19, s[36:39], s4 offen offset:2048 sc1 lds\n"
        "buffer_load_dwordx4 v19, s[36:39], s4 offen offset:3072 sc1 lds\n"
        "v_mov_b32 v22, 9\n"
        "s_waitcnt vmcnt(0)\n"
        "ds_write_b32 v18, v22\n"
        "s_cmp_eq_u32 s18, 0\n"
        "s_cbranch_scc1 Lnopoll_23\n"
        "s_branch Lspin_25\n"
        "Lsp_24:\n"
        "s_sleep 1\n"
        "s_sub_u32 s56, s56, 1\n"
        "s_cbranch_scc1 L_ABORT\n"
        "s_and_b32 s64, s56, 1023\n"
        "s_cbranch_scc0 Lvl_27\n"
        "Lspin_25:\n"
        "s_load_dwordx4 s[8:11], s[66:67], 0 glc\n"
        "Lspm_26:\n"
        "s_waitcnt lgkmcnt(0)\n"
        "s_cmp_eq_u32 s18, 1\n"
        "s_cselect_b64 s[10:11], s[8:9], s[10:11]\n"
        "s_and_b64 s[60:61], s[8:9], s[10:11]\n"
        "s_or_b64 s[64:65], s[8:9], s[10:11]\n"
        "s_or_b64 s[60:61], s[60:61], 7\n"
        "s_cmp_eq_u64 s[60:61], s[62:63]\n"
        "s_cbranch_scc0 Lsp_24\n"
        "s_or_b64 s[64:65], s[64:65], 15\n"
        "s_cmp_eq_u64 s[64:65], s[62:63]\n"
        "s_cbranch_scc0 Lsp_24\n"
        "Lnopoll_23:\n"
        "s_add_u32 m0, s74, 73728\n"
        "s_mov_b32 s4, 49152\n"
        "buffer_load_dwordx4 v19, s[36:39], s4 offen offset:0 sc1 lds\n"
        "buffer_load_dwordx4 v19, s[36:39], s4 offen offset:1024 sc1 lds\n"
        "buffer_load_dwordx4 v19, s[36:39], s4 offen offset:2048 sc1 lds\n"
        "buffer_load_dwordx4 v19, s[36:39], s4 offen offset:3072 sc1 lds\n"
        "s_add_u32 m0, s74, 77824\n"
        "s_mov_b32 s4, 53248\n"
        "buffer_load_dwordx4 v19, s[36:39], s4 offen offset:0 sc1 lds\n"
        "buffer_load_dwordx4 v19, s[36:39], s4 offen offset:1024 sc1 lds\n"
        "buffer_load_dwordx4 v19, s[36:39], s4 offen offset:2048 sc1 lds\n"
        "buffer_load_dwordx4 v19, s[36:39], s4 offen offset:3072 sc1 lds\n"
        "v_mov_b32 v22, 10\n"
        "s_waitcnt vmcnt(0)\n"
        "ds_write_b32 v18, v22\n"
        "s_setprio 0\n"
        "s_branch L_HTAIL\n"
        "L_WK2:\n"
        "s_add_u32 s66, s40, s79\n"
        "s_addc_u32 s67, s41, 0\n"
        "v_mov_b32 v23, 1\n"
        "s_cmp_eq_u32 s18, 0\n"
        "s_cbranch_scc1 L_EP1A\n"
        "s_load_dwordx4 s[8:11], s[66:67], 0 glc\n"
        "s_waitcnt lgkmcnt(0)\n"
        "s_cmp_eq_u32 s18, 1\n"
        "s_cselect_b64 s[10:11], s[8:9], s[10:11]\n"
        "s_and_b64 s[60:61], s[8:9], s[10:11]\n"
        "s_or_b64 s[64:65], s[8:9], s[10:11]\n"
        "s_or_b64 s[60:61], s[60:61], 14\n"
        "s_cmp_eq_u64 s[60:61], s[62:63]\n"
        "s_cbranch_scc0 L_EP1B\n"
        "s_or_b64 s[64:65], s[64:65], 15\n"
        "s_cmp_eq_u64 s[64:65], s[62:63]\n"
        "s_cbranch_scc0 L_EP1B\n"
        "L_EP1A:\n"
        "ds_write_b32 v202, v23\n"
        "v_mov_b32 v22, 6\n"
        "s_waitcnt vmcnt(0)\n"
        "ds_write_b32 v18, v22\n"
        "s_branch L_EP1C\n"
        "L_EP1B:\n"
        "v_mov_b32 v22, 6\n"
        "s_waitcnt vmcnt(0)\n"
        "ds_write_b32 v18, v22\n"
        "v_mov_b32 v23, 1\n"
        "s_cmp_eq_u32 s18, 0\n"
        "s_cbranch_scc1 Lnopoll_28\n"
        "s_branch Lspin_30\n"
        "Lsp_29:\n"
        "s_sleep 1\n"
        "s_sub_u32 s56, s56, 1\n"
        "s_cbranch_scc1 L_ABORT\n"
        "s_and_b32 s64, s56, 1023\n"
        "s_cbranch_scc0 Lvl_32\n"
        "Lspin_30:\n"
        "s_load_dwordx4 s[8:11], s[66:67], 0 glc\n"
        "Lspm_31:\n"
        "s_waitcnt lgkmcnt(0)\n"
        "s_cmp_eq_u32 s18, 1\n"
        "s_cselect_b64 s[10:11], s[8:9], s[10:11]\n"
        "s_and_b64 s[60:61], s[8:9], s[10:11]\n"
        "s_or_b64 s[64:65], s[8:9], s[10:11]\n"
        "s_or_b64 s[60:61], s[60:61], 14\n"
        "s_cmp_eq_u64 s[60:61], s[62:63]\n"
        "s_cbranch_scc0 Lsp_29\n"
        "s_or_b64 s[64:65], s[64:65], 15\n"
        "s_cmp_eq_u64 s[64:65], s[62:63]\n"
        "s_cbranch_scc0 Lsp_29\n"
        "Lnopoll_28:\n"
        "ds_write_b32 v202, v23\n"
        "L_EP1C:\n"
        "s_mov_b32 m0, 106496\n"
        "s_mov_b32 s4, 65536\n"
        "buffer_load_dwordx4 v19, s[36:39], s4 offen offset:0 sc1 lds\n"
        "buffer_load_dwordx4 v19, s[36:39], s4 offen offset:1024 sc1 lds\n"
        "buffer_load_dwordx4 v19, s[36:39], s4 offen offset:3072 sc1 lds\n"
        "s_mov_b32 m0, 110592\n"
        "s_mov_b32 s4, 69632\n"
        "buffer_load_dwordx4 v19, s[36:39], s4 offen offset:0 sc1 lds\n"
        "buffer_load_dwordx4 v19, s[36:39], s4 offen offset:1024 sc1 lds\n"
        "buffer_load_dwordx4 v19, s[36:39], s4 offen offset:3072 sc1 lds\n"
        "s_mov_b32 m0, 98304\n"
        "s_mov_b32 s4, 65536\n"
        "buffer_load_dwordx4 v19, s[32:35], s4 offen offset:0 sc1 lds\n"
        "buffer_load_dwordx4 v19, s[32:35], s4 offen offset:1024 sc1 lds\n"
        "buffer_load_dwordx4 v19, s[32:35], s4 offen offset:3072 sc1 lds\n"
        "s_mov_b32 m0, 102400\n"
        "s_mov_b32 s4, 69632\n"
        "buffer_load_dwordx4 v19, s[32:35], s4 offen offset:0 sc1 lds\n"
        "buffer_load_dwordx4 v19, s[32:35], s4 offen offset:1024 sc1 lds\n"
        "buffer_load_dwordx4 v19, s[32:35], s4 offen offset:3072 sc1 lds\n"
        "v_mov_b32 v22, 10\n"
        "s_waitcnt vmcnt(0)\n"
        "ds_write_b32 v18, v22\n"
        "s_setprio 0\n"
        "Lw_33:\n"
        "ds_read_b32 v20, v16\n"
        "s_waitcnt lgkmcnt(0)\n"
        "v_cmp_gt_u32 vcc, 27, v20\n"
        "s_cbranch_vccz Lwok_34\n"
        "s_sleep 1\n"
        "s_sub_u32 s56, s56, 1\n"
        "s_cbranch_scc1 L_ABORT\n"
        "s_branch Lw_33\n"
        "Lwok_34:\n"
        "Lw_35:\n"
        "ds_read_b32 v20, v204\n"
        "s_waitcnt lgkmcnt(0)\n"
        "v_cmp_gt_u32 vcc, 4, v20\n"
        "s_cbranch_vccz Lwok_36\n"
        "s_sleep 1\n"
        "s_sub_u32 s56, s56, 1\n"
        "s_cbranch_scc1 L_ABORT\n"
        "s_branch Lw_35\n"
        "Lwok_36:\n"
        "s_add_u32 m0, s77, 122880\n"
        "s_add_u32 s4, s76, 0\n"
        "s_cmp_lg_u32 s75, 0\n"
        "buffer_load_dwordx4 v26, s[44:47], s4 offen offset:0 lds\n"
        "buffer_load_dwordx4 v27, s[44:47], s4 offen offset:1024 lds\n"
        "s_cbranch_scc1 Lno3_37\n"
        "buffer_load_dwordx4 v28, s[44:47], s4 offen offset:2048 lds\n"
        "Lno3_37:\n"
        "s_add_u32 m0, s77, 125952\n"
        "s_add_u32 s4, s76, 82944\n"
        "s_cmp_lg_u32 s75, 0\n"
        "buffer_load_dwordx4 v26, s[44:47], s4 offen offset:0 lds\n"
        "buffer_load_dwordx4 v27, s[44:47], s4 offen offset:1024 lds\n"
        "s_cbranch_scc1 Lno3_38\n"
        "buffer_load_dwordx4 v28, s[44:47], s4 offen offset:2048 lds\n"
        "Lno3_38:\n"
        "s_add_u32 m0, s71, 147456\n"
        "s_cmp_lg_u32 s75, 0\n"
        "buffer_load_dwordx4 v29, s[44:47], s78 offen offset:0 lds\n"
        "buffer_load_dwordx4 v30, s[44:47], s78 offen offset:1024 lds\n"
        "s_cbranch_scc1 Lno3_39\n"
        "buffer_load_dwordx4 v31, s[44:47], s78 offen offset:2048 lds\n"
        "Lno3_39:\n"
        "s_branch L_HTAIL\n"
        "L_XWAVE:\n"
        "s_cmp_lg_u32 s20, 47\n"
        "s_cbranch_scc1 L_NOPR\n"
        "s_add_u32 s66, s16, 10452992\n"
        "s_addc_u32 s67, s17, 0\n"
        "v_and_b32 v1, 31, v0\n"
        "v_lshlrev_b32 v1, 4, v1\n"
        "v_lshrrev_b32 v2, 5, v0\n"
        "v_lshl_add_u32 v1, v2, 13, v1\n"
        "global_store_dwordx4 v1, v[4:7], s[66:67] sc1\n"
        "L_NOPR:\n"
        "s_add_u32 s68, s68, 128\n"
        "s_cmp_eq_u32 s68, 1152\n"
        "s_cselect_b32 s68, 0, s68\n"
        "s_add_u32 m0, s70, 73728\n"
        "s_nop 0\n"
        "buffer_load_dwordx4 v8, s[24:27], s68 offen offset:0 lds\n"
        "buffer_load_dwordx4 v9, s[24:27], s68 offen offset:1024 lds\n"
        "buffer_load_dwordx4 v10, s[24:27], s68 offen offset:2048 lds\n"
        "buffer_load_dwordx4 v11, s[24:27], s68 offen offset:3072 lds\n"
        "s_add_u32 s68, s68, 128\n"
        "s_cmp_eq_u32 s68, 1152\n"
        "s_cselect_b32 s68, 0, s68\n"
        "s_waitcnt vmcnt(4) lgkmcnt(0)\n"
        "s_barrier\n"
        "s_add_u32 s22, s22, 0x7f4a7c18\n"
        "s_addc_u32 s23, s23, 0x9e3779b9\n"
        "s_and_b32 s22, s22, -16\n"
        "s_or_b64 s[62:63], s[22:23], 15\n"
        "v_mov_b32 v207, s23\n"
        "s_add_u32 m0, s70, 24576\n"
        "s_nop 0\n"
        "buffer_load_dwordx4 v8, s[24:27], s68 offen offset:0 lds\n"
        "buffer_load_dwordx4 v9, s[24:27], s68 offen offset:1024 lds\n"
        "buffer_load_dwordx4 v10, s[24:27], s68 offen offset:2048 lds\n"
        "buffer_load_dwordx4 v11, s[24:27], s68 offen offset:3072 lds\n"
        "s_add_u32 s68, s68, 128\n"
        "s_cmp_eq_u32 s68, 1152\n"
        "s_cselect_b32 s68, 0, s68\n"
        "s_cmp_lg_u32 s20, 47\n"
        "s_cbranch_scc1 L_NOPR0\n"
        "s_memtime s[64:65]\n"
        "s_waitcnt lgkmcnt(0)\n"
        "s_load_dword s58, s[66:67], 0\n"
        "s_mov_b32 s28, s64\n"
        "s_waitcnt lgkmcnt(0)\n"
        "s_memtime s[64:65]\n"
        "s_waitcnt lgkmcnt(0)\n"
        "s_sub_u32 s28, s64, s28\n"
        "L_NOPR0:\n"
        "s_waitcnt vmcnt(4)\n"
        "s_barrier\n"
        "s_add_u32 m0, s70, 49152\n"
        "s_nop 0\n"
        "buffer_load_dwordx4 v8, s[24:27], s68 offen offset:0 lds\n"
        "buffer_load_dwordx4 v9, s[24:27], s68 offen offset:1024 lds\n"
        "buffer_load_dwordx4 v10, s[24:27], s68 offen offset:2048 lds\n"
        "buffer_load_dwordx4 v11, s[24:27], s68 offen offset:3072 lds\n"
        "s_add_u32 s68, s68, 128\n"
        "s_cmp_eq_u32 s68, 1152\n"
        "s_cselect_b32 s68, 0, s68\n"
        "s_cmp_lg_u32 s20, 47\n"
        "s_cbranch_scc1 L_NOPR1\n"
        "s_memtime s[64:65]\n"
        "s_waitcnt lgkmcnt(0)\n"
        "s_load_dword s58, s[66:67], 8320\n"
        "s_mov_b32 s29, s64\n"
        "s_waitcnt lgkmcnt(0)\n"
        "s_memtime s[64:65]\n"
        "s_waitcnt lgkmcnt(0)\n"
        "s_sub_u32 s29, s64, s29\n"
        "L_NOPR1:\n"
        "s_waitcnt vmcnt(4)\n"
        "s_barrier\n"
        "s_add_u32 m0, s70, 73728\n"
        "s_nop 0\n"
        "buffer_load_dwordx4 v8, s[24:27], s68 offen offset:0 lds\n"
        "buffer_load_dwordx4 v9, s[24:27], s68 offen offset:1024 lds\n"
        "buffer_load_dwordx4 v10, s[24:27], s68 offen offset:2048 lds\n"
        "buffer_load_dwordx4 v11, s[24:27], s68 offen offset:3072 lds\n"
        "s_add_u32 s68, s68, 128\n"
        "s_cmp_eq_u32 s68, 1152\n"
        "s_cselect_b32 s68, 0, s68\n"
        "s_cmp_lg_u32 s20, 47\n"
        "s_cbranch_scc1 L_NOPR2\n"
        "s_memtime s[64:65]\n"
        "s_waitcnt lgkmcnt(0)\n"
        "s_load_dword s58, s[66:67], 256\n"
        "s_mov_b32 s30, s64\n"
        "s_waitcnt lgkmcnt(0)\n"
        "s_memtime s[64:65]\n"
        "s_waitcnt lgkmcnt(0)\n"
        "s_sub_u32 s30, s64, s30\n"
        "L_NOPR2:\n"
        "s_waitcnt vmcnt(4)\n"
        "s_barrier\n"
        "s_cmp_lg_u32 s20, 47\n"
        "s_cbranch_scc1 L_NOPR3\n"
        "s_memtime s[64:65]\n"
        "s_waitcnt lgkmcnt(0)\n"
        "s_load_dword s58, s[66:67], 8576\n"
        "s_mov_b32 s31, s64\n"
        "s_waitcnt lgkmcnt(0)\n"
        "s_memtime s[64:65]\n"
        "s_waitcnt lgkmcnt(0)\n"
        "s_sub_u32 s31, s64, s31\n"
        "s_min_u32 s28, s28, s30\n"
        "s_min_u32 s29, s29, s31\n"
        "s_cmp_lt_u32 s29, s28\n"
        "s_cselect_b32 s57, 3, 1\n"
        "s_movk_i32 s58, 128\n"
        "s_add_u32 s7, s22, s57\n"
        "v_mov_b32 v206, s7\n"
        "buffer_store_dwordx2 v[206:207], v205, s[40:43], s58 offen sc1\n"
        "L_NOPR3:\n"
        "s_waitcnt vmcnt(0)\n"
        "s_barrier\n"
        "s_add_u32 m0, s70, 49152\n"
        "s_nop 0\n"
        "buffer_load_dwordx4 v8, s[24:27], s68 offen offset:0 lds\n"
        "buffer_load_dwordx4 v9, s[24:27], s68 offen offset:1024 lds\n"
        "buffer_load_dwordx4 v10, s[24:27], s68 offen offset:2048 lds\n"
        "buffer_load_dwordx4 v11, s[24:27], s68 offen offset:3072 lds\n"
        "s_add_u32 s68, s68, 128\n"
        "s_cmp_eq_u32 s68, 1152\n"
        "s_cselect_b32 s68, 0, s68\n"
        "s_barrier\n"
        "s_add_u32 m0, s70, 73728\n"
        "s_nop 0\n"
        "buffer_load_dwordx4 v8, s[24:27], s68 offen offset:0 lds\n"
        "buffer_load_dwordx4 v9, s[24:27], s68 offen offset:1024 lds\n"
        "buffer_load_dwordx4 v10, s[24:27], s68 offen offset:2048 lds\n"
        "buffer_load_dwordx4 v11, s[24:27], s68 offen offset:3072 lds\n"
        "s_add_u32 s68, s68, 128\n"
        "s_cmp_eq_u32 s68, 1152\n"
        "s_cselect_b32 s68, 0, s68\n"
        "s_waitcnt vmcnt(4)\n"
        "s_barrier\n"
        "s_barrier\n"
        "s_add_u32 m0, s70, 49152\n"
        "s_nop 0\n"
        "buffer_load_dwordx4 v8, s[24:27], s68 offen offset:0 lds\n"
        "buffer_load_dwordx4 v9, s[24:27], s68 offen offset:1024 lds\n"
        "buffer_load_dwordx4 v10, s[24:27], s68 offen offset:2048 lds\n"
        "buffer_load_dwordx4 v11, s[24:27], s68 offen offset:3072 lds\n"
        "s_add_u32 s68, s68, 128\n"
        "s_cmp_eq_u32 s68, 1152\n"
        "s_cselect_b32 s68, 0, s68\n"
        "s_waitcnt vmcnt(4)\n"
        "s_barrier\n"
        "s_load_dwordx2 s[8:9], s[16:17], 128\n"
        "s_barrier\n"
        "s_add_u32 m0, s70, 73728\n"
        "s_nop 0\n"
        "buffer_load_dwordx4 v8, s[24:27], s68 offen offset:0 lds\n"
        "buffer_load_dwordx4 v9, s[24:27], s68 offen offset:1024 lds\n"
        "buffer_load_dwordx4 v10, s[24:27], s68 offen offset:2048 lds\n"
        "buffer_load_dwordx4 v11, s[24:27], s68 offen offset:3072 lds\n"
        "s_add_u32 s28, s16, s83\n"
        "s_addc_u32 s29, s17, 0\n"
        "s_and_b32 s29, s29, 0xffff\n"
        "s_mov_b32 s30, 73728\n"
        "s_mov_b32 s31, 0x20000\n"
        "s_cmp_eq_u32 s18, 15\n"
        "s_cselect_b32 s30, 0, s30\n"
        "s_waitcnt lgkmcnt(0)\n"
        "Lpw_40:\n"
        "s_xor_b64 s[10:11], s[8:9], s[22:23]\n"
        "s_or_b32 s10, s10, 2\n"
        "s_cmp_eq_u64 s[10:11], 3\n"
        "s_cbranch_scc0 Lpws_41\n"
        "s_and_b32 s57, s8, 2\n"
        "s_lshl_b32 s57, s57, 12\n"
        "s_add_u32 s40, s40, s57\n"
        "s_addc_u32 s41, s41, 0\n"
        "s_add_u32 s32, s32, s57\n"
        "s_addc_u32 s33, s33, 0\n"
        "s_add_u32 s36, s36, s57\n"
        "s_addc_u32 s37, s37, 0\n"
        "s_add_u32 s48, s48, s57\n"
        "s_addc_u32 s49, s49, 0\n"
        "s_add_u32 s28, s28, s57\n"
        "s_addc_u32 s29, s29, 0\n"
        "s_add_u32 s57, s21, s59\n"
        "s_add_u32 s57, s57, 69632\n"
        "s_load_dword s12, s[40:41], s57 glc\n"
        "s_load_dword s13, s[40:41], s79 glc\n"
        "s_waitcnt vmcnt(4)\n"
        "s_barrier\n"
        "s_barrier\n"
        "s_waitcnt lgkmcnt(0)\n"
        "s_waitcnt vmcnt(0)\n"
        "s_barrier\n"
        "buffer_load_dword v50, v50, s[44:47], 0 offen\n"
        "buffer_load_dword v51, v51, s[44:47], 0 offen\n"
        "buffer_load_dword v52, v52, s[44:47], 0 offen\n"
        "s_barrier\n"
        "s_barrier\n"
        "ds_read_b128 v[88:91], v196 offset:0\n"
        "ds_read_b128 v[92:95], v196 offset:2048\n"
        "ds_read_b128 v[96:99], v196 offset:4096\n"
        "ds_read_b128 v[100:103], v196 offset:6144\n"
        "ds_read_b128 v[104:107], v198 offset:0\n"
        "ds_read_b128 v[108:111], v198 offset:4096\n"
        "s_mov_b32 s4, 0\n"
        "s_waitcnt lgkmcnt(5)\n"
        "buffer_store_dwordx4 v[88:91], v19, s[28:31], s4 offen offset:0 sc1\n"
        "s_waitcnt lgkmcnt(4)\n"
        "buffer_store_dwordx4 v[92:95], v19, s[28:31], s4 offen offset:2048 sc1\n"
        "s_mov_b32 s4, 4096\n"
        "s_waitcnt lgkmcnt(3)\n"
        "buffer_store_dwordx4 v[96:99], v19, s[28:31], s4 offen offset:0 sc1\n"
        "s_waitcnt lgkmcnt(2)\n"
        "buffer_store_dwordx4 v[100:103], v19, s[28:31], s4 offen offset:2048 sc1\n"
        "s_mov_b32 s4, 65536\n"
        "s_waitcnt lgkmcnt(1)\n"
        "buffer_store_dwordx4 v[104:107], v19, s[28:31], s4 offen offset:0 sc1\n"
        "s_mov_b32 s4, 69632\n"
        "s_waitcnt lgkmcnt(0)\n"
        "buffer_store_dwordx4 v[108:111], v19, s[28:31], s4 offen offset:0 sc1\n"
        "s_barrier\n"
        "s_barrier\n"
        "s_barrier\n"
        "s_barrier\n"
        "s_barrier\n"
        "s_barrier\n"
        "s_barrier\n"
        "s_waitcnt vmcnt(0)\n"
        "s_add_u32 s7, s22, 1\n"
        "v_mov_b32 v206, s7\n"
        "buffer_store_dwordx2 v[206:207], v24, s[40:43], s79 offen sc1\n"
        "v_mov_b32 v23, 1\n"
        "ds_write_b32 v204, v23\n"
        "v_mov_b32 v22, 8\n"
        "ds_write_b32 v18, v22\n"
        "s_sleep 4\n"
        "ds_read_b128 v[88:91], v196 offset:24576\n"
        "ds_read_b128 v[92:95], v196 offset:25600\n"
        "ds_read_b128 v[96:99], v196 offset:26624\n"
        "ds_read_b128 v[100:103], v196 offset:27648\n"
        "ds_read_b128 v[104:107], v196 offset:28672\n"
        "ds_read_b128 v[108:111], v196 offset:29696\n"
        "ds_read_b128 v[112:115], v196 offset:30720\n"
        "ds_read_b128 v[116:119], v196 offset:31744\n"
        "s_mov_b32 s4, 16384\n"
        "s_waitcnt lgkmcnt(7)\n"
        "buffer_store_dwordx4 v[88:91], v19, s[28:31], s4 offen offset:0 sc1\n"
        "s_waitcnt lgkmcnt(6)\n"
        "buffer_store_dwordx4 v[92:95], v19, s[28:31], s4 offen offset:1024 sc1\n"
        "s_waitcnt lgkmcnt(5)\n"
        "buffer_store_dwordx4 v[96:99], v19, s[28:31], s4 offen offset:2048 sc1\n"
        "s_waitcnt lgkmcnt(4)\n"
        "buffer_store_dwordx4 v[100:103], v19, s[28:31], s4 offen offset:3072 sc1\n"
        "s_mov_b32 s4, 20480\n"
        "s_waitcnt lgkmcnt(3)\n"
        "buffer_store_dwordx4 v[104:107], v19, s[28:31], s4 offen offset:0 sc1\n"
        "s_waitcnt lgkmcnt(2)\n"
        "buffer_store_dwordx4 v[108:111], v19, s[28:31], s4 offen offset:1024 sc1\n"
        "s_waitcnt lgkmcnt(1)\n"
        "buffer_store_dwordx4 v[112:115], v19, s[28:31], s4 offen offset:2048 sc1\n"
        "s_waitcnt lgkmcnt(0)\n"
        "buffer_store_dwordx4 v[116:119], v19, s[28:31], s4 offen offset:3072 sc1\n"
        "s_waitcnt vmcnt(0)\n"
        "s_add_u32 s7, s22, 3\n"
        "v_mov_b32 v206, s7\n"
        "buffer_store_dwordx2 v[206:207], v24, s[40:43], s79 offen sc1\n"
        "v_mov_b32 v23, 2\n"
        "ds_write_b32 v204, v23\n"
        "s_setprio 3\n"
        "Lw_42:\n"
        "ds_read_b32 v20, v202\n"
        "s_waitcnt lgkmcnt(0)\n"
        "v_cmp_gt_u32 vcc, 1, v20\n"
        "s_cbranch_vccz Lwok_43\n"
        "s_sleep 1\n"
        "s_sub_u32 s56, s56, 1\n"
        "s_cbranch_scc1 L_ABORT\n"
        "s_branch Lw_42\n"
        "Lwok_43:\n"
        "ds_read_b128 v[88:91], v197 offset:0\n"
        "ds_read_b128 v[92:95], v197 offset:1024\n"
        "ds_read_b128 v[96:99], v197 offset:2048\n"
        "ds_read_b128 v[100:103], v197 offset:3072\n"
        "ds_read_b128 v[104:107], v197 offset:4096\n"
        "ds_read_b128 v[108:111], v197 offset:5120\n"
        "ds_read_b128 v[112:115], v197 offset:6144\n"
        "ds_read_b128 v[116:119], v197 offset:7168\n"
        "s_mov_b32 s4, 32768\n"
        "s_waitcnt lgkmcnt(7)\n"
        "buffer_store_dwordx4 v[88:91], v19, s[28:31], s4 offen offset:0 sc1\n"
        "s_waitcnt lgkmcnt(6)\n"
        "buffer_store_dwordx4 v[92:95], v19, s[28:31], s4 offen offset:1024 sc1\n"
        "s_waitcnt lgkmcnt(5)\n"
        "buffer_store_dwordx4 v[96:99], v19, s[28:31], s4 offen offset:2048 sc1\n"
        "s_waitcnt lgkmcnt(4)\n"
        "buffer_store_dwordx4 v[100:103], v19, s[28:31], s4 offen offset:3072 sc1\n"
        "s_mov_b32 s4, 36864\n"
        "s_waitcnt lgkmcnt(3)\n"
        "buffer_store_dwordx4 v[104:107], v19, s[28:31], s4 offen offset:0 sc1\n"
        "s_waitcnt lgkmcnt(2)\n"
        "buffer_store_dwordx4 v[108:111], v19, s[28:31], s4 offen offset:1024 sc1\n"
        "s_waitcnt lgkmcnt(1)\n"
        "buffer_store_dwordx4 v[112:115], v19, s[28:31], s4 offen offset:2048 sc1\n"
        "s_waitcnt lgkmcnt(0)\n"
        "buffer_store_dwordx4 v[116:119], v19, s[28:31], s4 offen offset:3072 sc1\n"
        "ds_read_b128 v[120:123], v197 offset:24576\n"
        "ds_read_b128 v[124:127], v197 offset:25600\n"
        "ds_read_b128 v[128:131], v197 offset:26624\n"
        "ds_read_b128 v[132:135], v197 offset:27648\n"
        "ds_read_b128 v[136:139], v197 offset:28672\n"
        "ds_read_b128 v[140:143], v197 offset:29696\n"
        "ds_read_b128 v[144:147], v197 offset:30720\n"
        "ds_read_b128 v[148:151], v197 offset:31744\n"
        "s_mov_b32 s4, 49152\n"
        "s_waitcnt lgkmcnt(7)\n"
        "buffer_store_dwordx4 v[120:123], v19, s[28:31], s4 offen offset:0 sc1\n"
        "s_waitcnt lgkmcnt(6)\n"
        "buffer_store_dwordx4 v[124:127], v19, s[28:31], s4 offen offset:1024 sc1\n"
        "s_waitcnt lgkmcnt(5)\n"
        "buffer_store_dwordx4 v[128:131], v19, s[28:31], s4 offen offset:2048 sc1\n"
        "s_waitcnt lgkmcnt(4)\n"
        "buffer_store_dwordx4 v[132:135], v19, s[28:31], s4 offen offset:3072 sc1\n"
        "s_mov_b32 s4, 53248\n"
        "s_waitcnt lgkmcnt(3)\n"
        "buffer_store_dwordx4 v[136:139], v19, s[28:31], s4 offen offset:0 sc1\n"
        "s_waitcnt lgkmcnt(2)\n"
        "buffer_store_dwordx4 v[140:143], v19, s[28:31], s4 offen offset:1024 sc1\n"
        "s_waitcnt lgkmcnt(1)\n"
        "buffer_store_dwordx4 v[144:147], v19, s[28:31], s4 offen offset:2048 sc1\n"
        "s_waitcnt lgkmcnt(0)\n"
        "buffer_store_dwordx4 v[148:151], v19, s[28:31], s4 offen offset:3072 sc1\n"
        "v_mov_b32 v22, 10\n"
        "ds_write_b32 v18, v22\n"
        "s_waitcnt vmcnt(8)\n"
        "s_add_u32 s7, s22, 7\n"
        "v_mov_b32 v206, s7\n"
        "buffer_store_dwordx2 v[206:207], v24, s[40:43], s79 offen sc1\n"
        "v_mov_b32 v23, 3\n"
        "ds_write_b32 v204, v23\n"
        "s_waitcnt vmcnt(1)\n"
        "s_add_u32 s7, s22, 15\n"
        "v_mov_b32 v206, s7\n"
        "buffer_store_dwordx2 v[206:207], v24, s[40:43], s79 offen sc1\n"
        "v_mov_b32 v23, 4\n"
        "ds_write_b32 v204, v23\n"
        "s_setprio 0\n"
        "Lw_44:\n"
        "ds_read_b32 v20, v16\n"
        "s_waitcnt lgkmcnt(0)\n"
        "v_cmp_gt_u32 vcc, 27, v20\n"
        "s_cbranch_vccz Lwok_45\n"
        "s_sleep 1\n"
        "s_sub_u32 s56, s56, 1\n"
        "s_cbranch_scc1 L_ABORT\n"
        "s_branch Lw_44\n"
        "Lwok_45:\n"
        "s_add_u32 m0, s77, 122880\n"
        "s_add_u32 s4, s76, 0\n"
        "s_cmp_lg_u32 s75, 0\n"
        "buffer_load_dwordx4 v26, s[44:47], s4 offen offset:0 lds\n"
        "buffer_load_dwordx4 v27, s[44:47], s4 offen offset:1024 lds\n"
        "s_cbranch_scc1 Lno3_46\n"
        "buffer_load_dwordx4 v28, s[44:47], s4 offen offset:2048 lds\n"
        "Lno3_46:\n"
        "s_add_u32 m0, s77, 125952\n"
        "s_add_u32 s4, s76, 82944\n"
        "s_cmp_lg_u32 s75, 0\n"
        "buffer_load_dwordx4 v26, s[44:47], s4 offen offset:0 lds\n"
        "buffer_load_dwordx4 v27, s[44:47], s4 offen offset:1024 lds\n"
        "s_cbranch_scc1 Lno3_47\n"
        "buffer_load_dwordx4 v28, s[44:47], s4 offen offset:2048 lds\n"
        "Lno3_47:\n"
        "s_add_u32 m0, s71, 147456\n"
        "s_cmp_lg_u32 s75, 0\n"
        "buffer_load_dwordx4 v29, s[44:47], s78 offen offset:0 lds\n"
        "buffer_load_dwordx4 v30, s[44:47], s78 offen offset:1024 lds\n"
        "s_cbranch_scc1 Lno3_48\n"
        "buffer_load_dwordx4 v31, s[44:47], s78 offen offset:2048 lds\n"
        "Lno3_48:\n"
        "L_HTAIL:\n"
        "s_setprio 0\n"
        "s_add_u32 s57, s21, s59\n"
        "s_add_u32 s57, s57, 69632\n"
        "s_mov_b32 s68, 1\n"
        "s_add_u32 s66, s40, s57\n"
        "s_addc_u32 s67, s41, 0\n"
        "Lw_49:\n"
        "ds_read_b32 v20, v16\n"
        "s_waitcnt lgkmcnt(0)\n"
        "v_cmp_gt_u32 vcc, 28, v20\n"
        "s_cbranch_vccz Lwok_50\n"
        "s_sleep 1\n"
        "s_sub_u32 s56, s56, 1\n"
        "s_cbranch_scc1 L_ABORT\n"
        "s_branch Lw_49\n"
        "Lwok_50:\n"
        "s_sleep 6\n"
        "s_add_u32 m0, s77, 0\n"
        "s_add_u32 s4, s76, 23040\n"
        "s_cmp_lg_u32 s75, 0\n"
        "buffer_load_dwordx4 v26, s[44:47], s4 offen offset:0 lds\n"
        "buffer_load_dwordx4 v27, s[44:47], s4 offen offset:1024 lds\n"
        "s_cbranch_scc1 Lno3_51\n"
        "buffer_load_dwordx4 v28, s[44:47], s4 offen offset:2048 lds\n"
        "Lno3_51:\n"
        "s_add_u32 m0, s77, 3072\n"
        "s_add_u32 s4, s76, 105984\n"
        "s_cmp_lg_u32 s75, 0\n"
        "buffer_load_dwordx4 v26, s[44:47], s4 offen offset:0 lds\n"
        "buffer_load_dwordx4 v27, s[44:47], s4 offen offset:1024 lds\n"
        "s_cbranch_scc1 Lno3_52\n"
        "buffer_load_dwordx4 v28, s[44:47], s4 offen offset:2048 lds\n"
        "Lno3_52:\n"
        "s_branch Lspin_54\n"
        "Lsp_53:\n"
        "s_sleep 1\n"
        "s_sub_u32 s56, s56, 1\n"
        "s_cbranch_scc1 L_ABORT\n"
        "s_and_b32 s64, s56, 1023\n"
        "s_cbranch_scc0 Lvl_56\n"
        "Lspin_54:\n"
        "s_load_dwordx16 s[24:39], s[66:67], 0 glc\n"
        "Lspm_55:\n"
        "s_waitcnt lgkmcnt(0)\n"
        "s_and_b64 s[60:61], s[24:25], s[26:27]\n"
        "s_or_b64 s[64:65], s[24:25], s[26:27]\n"
        "s_and_b64 s[60:61], s[60:61], s[28:29]\n"
        "s_or_b64 s[64:65], s[64:65], s[28:29]\n"
        "s_and_b64 s[60:61], s[60:61], s[30:31]\n"
        "s_or_b64 s[64:65], s[64:65], s[30:31]\n"
        "s_and_b64 s[60:61], s[60:61], s[32:33]\n"
        "s_or_b64 s[64:65], s[64:65], s[32:33]\n"
        "s_and_b64 s[60:61], s[60:61], s[34:35]\n"
        "s_or_b64 s[64:65], s[64:65], s[34:35]\n"
        "s_and_b64 s[60:61], s[60:61], s[36:37]\n"
        "s_or_b64 s[64:65], s[64:65], s[36:37]\n"
        "s_and_b64 s[60:61], s[60:61], s[38:39]\n"
        "s_or_b64 s[64:65], s[64:65], s[38:39]\n"
        "s_or_b64 s[60:61], s[60:61], 14\n"
        "s_cmp_eq_u64 s[60:61], s[62:63]\n"
        "s_cbranch_scc0 Lsp_53\n"
        "s_or_b64 s[64:65], s[64:65], 15\n"
        "s_cmp_eq_u64 s[64:65], s[62:63]\n"
        "s_cbranch_scc0 Lsp_53\n"
        "s_mov_b32 s4, 0\n"
        "s_mov_b32 s7, 4096\n"
        "s_mov_b32 s57, 65536\n"
        "buffer_load_dwordx4 v[88:91], v19, s[48:51], s4 offen offset:0 sc1\n"
        "buffer_load_dwordx4 v[92:95], v19, s[48:51], s4 offen offset:1024 sc1\n"
        "buffer_load_dwordx4 v[96:99], v19, s[48:51], s4 offen offset:2048 sc1\n"
        "buffer_load_dwordx4 v[100:103], v19, s[48:51], s4 offen offset:3072 sc1\n"
        "buffer_load_dwordx4 v[104:107], v19, s[48:51], s7 offen offset:0 sc1\n"
        "buffer_load_dwordx4 v[108:111], v19, s[48:51], s7 offen offset:1024 sc1\n"
        "buffer_load_dwordx4 v[112:115], v19, s[48:51], s7 offen offset:2048 sc1\n"
        "buffer_load_dwordx4 v[116:119], v19, s[48:51], s7 offen offset:3072 sc1\n"
        "buffer_load_dword v120, v15, s[48:51], s57 offen offset:0 sc1\n"
        "buffer_load_dword v121, v15, s[48:51], s57 offen offset:128 sc1\n"
        "buffer_load_dword v122, v15, s[48:51], s57 offen offset:256 sc1\n"
        "buffer_load_dword v123, v15, s[48:51], s57 offen offset:384 sc1\n"
        "v_mov_b32 v22, 11\n"
        "s_waitcnt vmcnt(12)\n"
        "ds_write_b32 v18, v22\n"
        "Lw_57:\n"
        "ds_read_b32 v20, v16\n"
        "s_waitcnt lgkmcnt(0)\n"
        "v_cmp_gt_u32 vcc, 29, v20\n"
        "s_cbranch_vccz Lwok_58\n"
        "s_sleep 1\n"
        "s_sub_u32 s56, s56, 1\n"
        "s_cbranch_scc1 L_ABORT\n"
        "s_branch Lw_57\n"
        "Lwok_58:\n"
        "s_sleep 6\n"
        "s_add_u32 m0, s77, 24576\n"
        "s_add_u32 s4, s76, 41472\n"
        "s_cmp_lg_u32 s75, 0\n"
        "buffer_load_dwordx4 v26, s[44:47], s4 offen offset:0 lds\n"
        "buffer_load_dwordx4 v27, s[44:47], s4 offen offset:1024 lds\n"
        "s_cbranch_scc1 Lno3_59\n"
        "buffer_load_dwordx4 v28, s[44:47], s4 offen offset:2048 lds\n"
        "Lno3_59:\n"
        "s_add_u32 m0, s77, 27648\n"
        "s_add_u32 s4, s76, 124416\n"
        "s_cmp_lg_u32 s75, 0\n"
        "buffer_load_dwordx4 v26, s[44:47], s4 offen offset:0 lds\n"
        "buffer_load_dwordx4 v27, s[44:47], s4 offen offset:1024 lds\n"
        "s_cbranch_scc1 Lno3_60\n"
        "buffer_load_dwordx4 v28, s[44:47], s4 offen offset:2048 lds\n"
        "Lno3_60:\n"
        "Lw_61:\n"
        "ds_read_b32 v20, v17\n"
        "s_waitcnt lgkmcnt(0)\n"
        "v_cmp_gt_u32 vcc, 11, v20\n"
        "s_cbranch_vccz Lwok_62\n"
        "s_sleep 1\n"
        "s_sub_u32 s56, s56, 1\n"
        "s_cbranch_scc1 L_ABORT\n"
        "s_branch Lw_61\n"
        "Lwok_62:\n"
        "s_branch Lspin_64\n"
        "Lsp_63:\n"
        "s_sleep 1\n"
        "s_sub_u32 s56, s56, 1\n"
        "s_cbranch_scc1 L_ABORT\n"
        "s_and_b32 s64, s56, 1023\n"
        "s_cbranch_scc0 Lvl_66\n"
        "Lspin_64:\n"
        "s_load_dwordx16 s[24:39], s[66:67], 0 glc\n"
        "Lspm_65:\n"
        "s_waitcnt lgkmcnt(0)\n"
        "s_and_b64 s[60:61], s[24:25], s[26:27]\n"
        "s_or_b64 s[64:65], s[24:25], s[26:27]\n"
        "s_and_b64 s[60:61], s[60:61], s[28:29]\n"
        "s_or_b64 s[64:65], s[64:65], s[28:29]\n"
        "s_and_b64 s[60:61], s[60:61], s[30:31]\n"
        "s_or_b64 s[64:65], s[64:65], s[30:31]\n"
        "s_and_b64 s[60:61], s[60:61], s[32:33]\n"
        "s_or_b64 s[64:65], s[64:65], s[32:33]\n"
        "s_and_b64 s[60:61], s[60:61], s[34:35]\n"
        "s_or_b64 s[64:65], s[64:65], s[34:35]\n"
        "s_and_b64 s[60:61], s[60:61], s[36:37]\n"
        "s_or_b64 s[64:65], s[64:65], s[36:37]\n"
        "s_and_b64 s[60:61], s[60:61], s[38:39]\n"
        "s_or_b64 s[64:65], s[64:65], s[38:39]\n"
        "s_or_b64 s[60:61], s[60:61], 13\n"
        "s_cmp_eq_u64 s[60:61], s[62:63]\n"
        "s_cbranch_scc0 Lsp_63\n"
        "s_or_b64 s[64:65], s[64:65], 15\n"
        "s_cmp_eq_u64 s[64:65], s[62:63]\n"
        "s_cbranch_scc0 Lsp_63\n"
        "s_mov_b32 s4, 16384\n"
        "s_mov_b32 s7, 20480\n"
        "s_mov_b32 s57, 66560\n"
        "buffer_load_dwordx4 v[124:127], v19, s[48:51], s4 offen offset:0 sc1\n"
        "buffer_load_dwordx4 v[128:131], v19, s[48:51], s4 offen offset:1024 sc1\n"
        "buffer_load_dwordx4 v[132:135], v19, s[48:51], s4 offen offset:2048 sc1\n"
        "buffer_load_dwordx4 v[136:139], v19, s[48:51], s4 offen offset:3072 sc1\n"
        "buffer_load_dwordx4 v[140:143], v19, s[48:51], s7 offen offset:0 sc1\n"
        "buffer_load_dwordx4 v[144:147], v19, s[48:51], s7 offen offset:1024 sc1\n"
        "buffer_load_dwordx4 v[148:151], v19, s[48:51], s7 offen offset:2048 sc1\n"
        "buffer_load_dwordx4 v[152:155], v19, s[48:51], s7 offen offset:3072 sc1\n"
        "buffer_load_dword v156, v15, s[48:51], s57 offen offset:0 sc1\n"
        "buffer_load_dword v157, v15, s[48:51], s57 offen offset:128 sc1\n"
        "buffer_load_dword v158, v15, s[48:51], s57 offen offset:256 sc1\n"
        "buffer_load_dword v159, v15, s[48:51], s57 offen offset:384 sc1\n"
        "Lw_67:\n"
        "ds_read_b32 v20, v16\n"
        "s_waitcnt lgkmcnt(0)\n"
        "v_cmp_gt_u32 vcc, 30, v20\n"
        "s_cbranch_vccz Lwok_68\n"
        "s_sleep 1\n"
        "s_sub_u32 s56, s56, 1\n"
        "s_cbranch_scc1 L_ABORT\n"
        "s_branch Lw_67\n"
        "Lwok_68:\n"
        "s_sleep 6\n"
        "s_add_u32 m0, s77, 49152\n"
        "s_add_u32 s4, s76, 64512\n"
        "s_cmp_lg_u32 s75, 0\n"
        "buffer_load_dwordx4 v26, s[44:47], s4 offen offset:0 lds\n"
        "buffer_load_dwordx4 v27, s[44:47], s4 offen offset:1024 lds\n"
        "s_cbranch_scc1 Lno3_69\n"
        "buffer_load_dwordx4 v28, s[44:47], s4 offen offset:2048 lds\n"
        "Lno3_69:\n"
        "s_add_u32 m0, s77, 52224\n"
        "s_add_u32 s4, s76, 147456\n"
        "s_cmp_lg_u32 s75, 0\n"
        "buffer_load_dwordx4 v26, s[44:47], s4 offen offset:0 lds\n"
        "buffer_load_dwordx4 v27, s[44:47], s4 offen offset:1024 lds\n"
        "s_cbranch_scc1 Lno3_70\n"
        "buffer_load_dwordx4 v28, s[44:47], s4 offen offset:2048 lds\n"
        "Lno3_70:\n"
        "s_cmp_eq_u32 s75, 0\n"
        "s_cbranch_scc1 L_OP3\n"
        "s_mov_b32 s4, 32768\n"
        "s_mov_b32 s7, 36864\n"
        "s_mov_b32 s57, 67584\n"
        "ds_read_b64_tr_b16 v[56:57], v35 offset:0\n"
        "ds_read_b64_tr_b16 v[58:59], v35 offset:1024\n"
        "ds_read_b64_tr_b16 v[60:61], v36 offset:0\n"
        "ds_read_b64_tr_b16 v[62:63], v36 offset:1024\n"
        "ds_read_b64_tr_b16 v[64:65], v35 offset:3072\n"
        "ds_read_b64_tr_b16 v[66:67], v35 offset:4096\n"
        "ds_read_b64_tr_b16 v[68:69], v36 offset:3072\n"
        "ds_read_b64_tr_b16 v[70:71], v36 offset:4096\n"
        "ds_read_b64_tr_b16 v[72:73], v35 offset:6144\n"
        "ds_read_b64_tr_b16 v[74:75], v35 offset:7168\n"
        "v_mov_b32 v22, 12\n"
        "s_waitcnt vmcnt(20)\n"
        "Lw_71:\n"
        "ds_read_b32 v20, v53\n"
        "s_waitcnt lgkmcnt(0)\n"
        "v_cmp_gt_u32 vcc, 31, v20\n"
        "s_cbranch_vccz Lwok_72\n"
        "s_sleep 1\n"
        "s_sub_u32 s56, s56, 1\n"
        "s_cbranch_scc1 L_ABORT\n"
        "s_branch Lw_71\n"
        "Lwok_72:\n"
        "v_mfma_f32_32x32x16_bf16 v[208:223], v[56:59], v[88:91], 0\n"
        "ds_read_b64_tr_b16 v[76:77], v36 offset:6144\n"
        "ds_read_b64_tr_b16 v[78:79], v36 offset:7168\n"
        "v_mfma_f32_32x32x16_bf16 v[224:239], v[60:63], v[88:91], 0\n"
        "ds_read_b64_tr_b16 v[80:81], v35 offset:9216\n"
        "ds_read_b64_tr_b16 v[82:83], v35 offset:10240\n"
        "v_mfma_f32_32x32x16_bf16 v[208:223], v[64:67], v[92:95], v[208:223]\n"
        "ds_read_b64_tr_b16 v[84:85], v36 offset:9216\n"
        "ds_read_b64_tr_b16 v[86:87], v36 offset:10240\n"
        "v_mfma_f32_32x32x16_bf16 v[224:239], v[68:71], v[92:95], v[224:239]\n"
        "ds_read_b64_tr_b16 v[56:57], v35 offset:12288\n"
        "ds_read_b64_tr_b16 v[58:59], v35 offset:13312\n"
        "v_mfma_f32_32x32x16_bf16 v[208:223], v[72:75], v[96:99], v[208:223]\n"
        "ds_read_b64_tr_b16 v[60:61], v36 offset:12288\n"
        "ds_read_b64_tr_b16 v[62:63], v36 offset:13312\n"
        "s_waitcnt lgkmcnt(8)\n"
        "v_mfma_f32_32x32x16_bf16 v[224:239], v[76:79], v[96:99], v[224:239]\n"
        "ds_read_b64_tr_b16 v[64:65], v35 offset:15360\n"
        "ds_read_b64_tr_b16 v[66:67], v35 offset:16384\n"
        "s_waitcnt lgkmcnt(8)\n"
        "v_mfma_f32_32x32x16_bf16 v[208:223], v[80:83], v[100:103], v[208:223]\n"
        "ds_read_b64_tr_b16 v[68:69], v36 offset:15360\n"
        "ds_read_b64_tr_b16 v[70:71], v36 offset:16384\n"
        "s_waitcnt lgkmcnt(8)\n"
        "v_mfma_f32_32x32x16_bf16 v[224:239], v[84:87], v[100:103], v[224:239]\n"
        "ds_read_b64_tr_b16 v[72:73], v35 offset:18432\n"
        "ds_read_b64_tr_b16 v[74:75], v35 offset:19456\n"
        "s_waitcnt lgkmcnt(8)\n"
        "v_mfma_f32_32x32x16_bf16 v[208:223], v[56:59], v[104:107], v[208:223]\n"
        "ds_read_b64_tr_b16 v[76:77], v36 offset:18432\n"
        "ds_read_b64_tr_b16 v[78:79], v36 offset:19456\n"
        "s_waitcnt lgkmcnt(8)\n"
        "v_mfma_f32_32x32x16_bf16 v[224:239], v[60:63], v[104:107], v[224:239]\n"
        "ds_read_b64_tr_b16 v[80:81], v35 offset:21504\n"
        "ds_read_b64_tr_b16 v[82:83], v35 offset:22528\n"
        "s_waitcnt lgkmcnt(8)\n"
        "v_mfma_f32_32x32x16_bf16 v[208:223], v[64:67], v[108:111], v[208:223]\n"
        "ds_read_b64_tr_b16 v[84:85], v36 offset:21504\n"
        "ds_read_b64_tr_b16 v[86:87], v36 offset:22528\n"
        "s_waitcnt lgkmcnt(8)\n"
        "v_mfma_f32_32x32x16_bf16 v[224:239], v[68:71], v[108:111], v[224:239]\n"
        "ds_read_b64_tr_b16 v[56:57], v35 offset:24576\n"
        "ds_read_b64_tr_b16 v[58:59], v35 offset:25600\n"
        "s_waitcnt lgkmcnt(8)\n"
        "v_mfma_f32_32x32x16_bf16 v[208:223], v[72:75], v[112:115], v[208:223]\n"
        "ds_read_b64_tr_b16 v[60:61], v36 offset:24576\n"
        "ds_read_b64_tr_b16 v[62:63], v36 offset:25600\n"
        "s_waitcnt lgkmcnt(8)\n"
        "v_mfma_f32_32x32x16_bf16 v[224:239], v[76:79], v[112:115], v[224:239]\n"
        "ds_read_b64_tr_b16 v[64:65], v32 offset:0\n"
        "ds_read_b64_tr_b16 v[66:67], v32 offset:1024\n"
        "s_waitcnt lgkmcnt(8)\n"
        "v_mfma_f32_32x32x16_bf16 v[208:223], v[80:83], v[116:119], v[208:223]\n"
        "ds_read_b64_tr_b16 v[68:69], v33 offset:0\n"
        "ds_read_b64_tr_b16 v[70:71], v33 offset:1024\n"
        "ds_read_b32 v20, v53\n"
        "s_waitcnt lgkmcnt(9)\n"
        "v_mfma_f32_32x32x16_bf16 v[224:239], v[84:87], v[116:119], v[224:239]\n"
        "ds_read_b64_tr_b16 v[72:73], v32 offset:3072\n"
        "ds_read_b64_tr_b16 v[74:75], v32 offset:4096\n"
        "s_waitcnt lgkmcnt(9)\n"
        "v_mfma_f32_32x32x16_bf16 v[208:223], v[56:59], v[120:123], v[208:223]\n"
        "ds_read_b64_tr_b16 v[76:77], v33 offset:3072\n"
        "ds_read_b64_tr_b16 v[78:79], v33 offset:4096\n"
        "s_waitcnt vmcnt(0)\n"
        "ds_write_b32 v18, v22\n"
        "s_waitcnt lgkmcnt(5)\n"
        "v_cmp_gt_u32 s[60:61], 33, v20\n"
        "v_mfma_f32_32x32x16_bf16 v[224:239], v[60:63], v[120:123], v[224:239]\n"
        "ds_read_b64_tr_b16 v[80:81], v32 offset:6144\n"
        "ds_read_b64_tr_b16 v[82:83], v32 offset:7168\n"
        "s_andn2_b32 s58, s68, s60\n"
        "s_cmp_eq_u32 s58, 0\n"
        "s_cbranch_scc1 Lskip_73\n"
        "buffer_load_dwordx4 v[160:163], v19, s[48:51], s4 offen offset:0 sc1\n"
        "Lskip_73:\n"
        "v_mfma_f32_32x32x16_bf16 v[208:223], v[64:67], v[124:127], v[208:223]\n"
        "ds_read_b64_tr_b16 v[84:85], v33 offset:6144\n"
        "ds_read_b64_tr_b16 v[86:87], v33 offset:7168\n"
        "s_cbranch_scc1 Lskip_74\n"
        "buffer_load_dwordx4 v[164:167], v19, s[48:51], s4 offen offset:1024 sc1\n"
        "Lskip_74:\n"
        "v_mfma_f32_32x32x16_bf16 v[224:239], v[68:71], v[124:127], v[224:239]\n"
        "ds_read_b64_tr_b16 v[56:57], v32 offset:9216\n"
        "ds_read_b64_tr_b16 v[58:59], v32 offset:10240\n"
        "s_cbranch_scc1 Lskip_75\n"
        "buffer_load_dwordx4 v[168:171], v19, s[48:51], s4 offen offset:2048 sc1\n"
        "Lskip_75:\n"
        "s_waitcnt lgkmcnt(9)\n"
        "v_mfma_f32_32x32x16_bf16 v[208:223], v[72:75], v[128:131], v[208:223]\n"
        "ds_read_b64_tr_b16 v[60:61], v33 offset:9216\n"
        "ds_read_b64_tr_b16 v[62:63], v33 offset:10240\n"
        "s_cbranch_scc1 Lskip_76\n"
        "buffer_load_dwordx4 v[172:175], v19, s[48:51], s4 offen offset:3072 sc1\n"
        "Lskip_76:\n"
        "s_waitcnt lgkmcnt(9)\n"
        "v_mfma_f32_32x32x16_bf16 v[224:239], v[76:79], v[128:131], v[224:239]\n"
        "ds_read_b64_tr_b16 v[64:65], v32 offset:12288\n"
        "ds_read_b64_tr_b16 v[66:67], v32 offset:13312\n"
        "s_cbranch_scc1 Lskip_77\n"
        "buffer_load_dwordx4 v[176:179], v19, s[48:51], s7 offen offset:0 sc1\n"
        "Lskip_77:\n"
        "s_waitcnt lgkmcnt(8)\n"
        "v_mfma_f32_32x32x16_bf16 v[208:223], v[80:83], v[132:135], v[208:223]\n"
        "ds_read_b64_tr_b16 v[68:69], v33 offset:12288\n"
        "ds_read_b64_tr_b16 v[70:71], v33 offset:13312\n"
        "s_cbranch_scc1 Lskip_78\n"
        "buffer_load_dwordx4 v[180:183], v19, s[48:51], s7 offen offset:1024 sc1\n"
        "Lskip_78:\n"
        "s_waitcnt lgkmcnt(8)\n"
        "v_mfma_f32_32x32x16_bf16 v[224:239], v[84:87], v[132:135], v[224:239]\n"
        "ds_read_b64_tr_b16 v[72:73], v32 offset:15360\n"
        "ds_read_b64_tr_b16 v[74:75], v32 offset:16384\n"
        "s_cbranch_scc1 Lskip_79\n"
        "buffer_load_dwordx4 v[184:187], v19, s[48:51], s7 offen offset:2048 sc1\n"
        "Lskip_79:\n"
        "s_waitcnt lgkmcnt(8)\n"
        "v_mfma_f32_32x32x16_bf16 v[208:223], v[56:59], v[136:139], v[208:223]\n"
        "ds_read_b64_tr_b16 v[76:77], v33 offset:15360\n"
        "ds_read_b64_tr_b16 v[78:79], v33 offset:16384\n"
        "s_cbranch_scc1 Lskip_80\n"
        "buffer_load_dwordx4 v[188:191], v19, s[48:51], s7 offen offset:3072 sc1\n"
        "Lskip_80:\n"
        "s_waitcnt lgkmcnt(8)\n"
        "v_mfma_f32_32x32x16_bf16 v[224:239], v[60:63], v[136:139], v[224:239]\n"
        "ds_read_b64_tr_b16 v[80:81], v32 offset:18432\n"
        "ds_read_b64_tr_b16 v[82:83], v32 offset:19456\n"
        "s_cbranch_scc1 Lskip_81\n"
        "buffer_load_dword v192, v15, s[48:51], s57 offen offset:0 sc1\n"
        "Lskip_81:\n"
        "s_waitcnt lgkmcnt(8)\n"
        "v_mfma_f32_32x32x16_bf16 v[208:223], v[64:67], v[140:143], v[208:223]\n"
        "ds_read_b64_tr_b16 v[84:85], v33 offset:18432\n"
        "ds_read_b64_tr_b16 v[86:87], v33 offset:19456\n"
        "ds_read_b32 v20, v17\n"
        "s_cbranch_scc1 Lskip_82\n"
        "buffer_load_dword v193, v15, s[48:51], s57 offen offset:128 sc1\n"
        "Lskip_82:\n"
        "s_waitcnt lgkmcnt(9)\n"
        "v_mfma_f32_32x32x16_bf16 v[224:239], v[68:71], v[140:143], v[224:239]\n"
        "ds_read_b64_tr_b16 v[56:57], v32 offset:21504\n"
        "ds_read_b64_tr_b16 v[58:59], v32 offset:22528\n"
        "s_cbranch_scc1 Lskip_83\n"
        "buffer_load_dword v194, v15, s[48:51], s57 offen offset:256 sc1\n"
        "Lskip_83:\n"
        "s_waitcnt lgkmcnt(9)\n"
        "v_mfma_f32_32x32x16_bf16 v[208:223], v[72:75], v[144:147], v[208:223]\n"
        "ds_read_b64_tr_b16 v[60:61], v33 offset:21504\n"
        "ds_read_b64_tr_b16 v[62:63], v33 offset:22528\n"
        "s_cbranch_scc1 Lskip_84\n"
        "buffer_load_dword v195, v15, s[48:51], s57 offen offset:384 sc1\n"
        "Lskip_84:\n"
        "s_waitcnt lgkmcnt(9)\n"
        "v_mfma_f32_32x32x16_bf16 v[224:239], v[76:79], v[144:147], v[224:239]\n"
        "ds_read_b64_tr_b16 v[64:65], v35 offset:27648\n"
        "ds_read_b64_tr_b16 v[66:67], v35 offset:28672\n"
        "s_waitcnt lgkmcnt(6)\n"
        "v_cmp_gt_u32 vcc, 12, v20\n"
        "v_mfma_f32_32x32x16_bf16 v[208:223], v[80:83], v[148:151], v[208:223]\n"
        "s_cbranch_vccnz Lslow_85\n"
        "Lback_86:\n"
        "ds_read_b64_tr_b16 v[68:69], v36 offset:27648\n"
        "ds_read_b64_tr_b16 v[70:71], v36 offset:28672\n"
        "v_mfma_f32_32x32x16_bf16 v[224:239], v[84:87], v[148:151], v[224:239]\n"
        "ds_read_b64_tr_b16 v[72:73], v32 offset:24576\n"
        "ds_read_b64_tr_b16 v[74:75], v32 offset:25600\n"
        "s_waitcnt lgkmcnt(8)\n"
        "v_mfma_f32_32x32x16_bf16 v[208:223], v[56:59], v[152:155], v[208:223]\n"
        "ds_read_b64_tr_b16 v[76:77], v33 offset:24576\n"
        "ds_read_b64_tr_b16 v[78:79], v33 offset:25600\n"
        "s_waitcnt lgkmcnt(8)\n"
        "v_mfma_f32_32x32x16_bf16 v[224:239], v[60:63], v[152:155], v[224:239]\n"
        "ds_read_b64_tr_b16 v[80:81], v32 offset:27648\n"
        "ds_read_b64_tr_b16 v[82:83], v32 offset:28672\n"
        "s_waitcnt lgkmcnt(8)\n"
        "v_mfma_f32_32x32x16_bf16 v[208:223], v[64:67], v[156:159], v[208:223]\n"
        "ds_read_b64_tr_b16 v[84:85], v33 offset:27648\n"
        "ds_read_b64_tr_b16 v[86:87], v33 offset:28672\n"
        "s_waitcnt lgkmcnt(8)\n"
        "v_mfma_f32_32x32x16_bf16 v[224:239], v[68:71], v[156:159], v[224:239]\n"
        "ds_read_b64_tr_b16 v[56:57], v32 offset:30720\n"
        "ds_read_b64_tr_b16 v[58:59], v32 offset:31744\n"
        "s_and_b32 s60, s60, s68\n"
        "s_cbranch_scc0 Lhave2_87\n"
        "Lw_88:\n"
        "ds_read_b32 v20, v53\n"
        "s_waitcnt lgkmcnt(0)\n"
        "v_cmp_gt_u32 vcc, 33, v20\n"
        "s_cbranch_vccz Lwok_89\n"
        "s_sleep 0\n"
        "s_sub_u32 s56, s56, 1\n"
        "s_cbranch_scc1 L_ABORT\n"
        "s_branch Lw_88\n"
        "Lwok_89:\n"
        "buffer_load_dwordx4 v[160:163], v19, s[48:51], s4 offen offset:0 sc1\n"
        "buffer_load_dwordx4 v[164:167], v19, s[48:51], s4 offen offset:1024 sc1\n"
        "buffer_load_dwordx4 v[168:171], v19, s[48:51], s4 offen offset:2048 sc1\n"
        "buffer_load_dwordx4 v[172:175], v19, s[48:51], s4 offen offset:3072 sc1\n"
        "buffer_load_dwordx4 v[176:179], v19, s[48:51], s7 offen offset:0 sc1\n"
        "buffer_load_dwordx4 v[180:183], v19, s[48:51], s7 offen offset:1024 sc1\n"
        "buffer_load_dwordx4 v[184:187], v19, s[48:51], s7 offen offset:2048 sc1\n"
        "buffer_load_dwordx4 v[188:191], v19, s[48:51], s7 offen offset:3072 sc1\n"
        "buffer_load_dword v192, v15, s[48:51], s57 offen offset:0 sc1\n"
        "buffer_load_dword v193, v15, s[48:51], s57 offen offset:128 sc1\n"
        "buffer_load_dword v194, v15, s[48:51], s57 offen offset:256 sc1\n"
        "buffer_load_dword v195, v15, s[48:51], s57 offen offset:384 sc1\n"
        "Lhave2_87:\n"
        "s_waitcnt vmcnt(11)\n"
        "s_waitcnt lgkmcnt(8)\n"
        "v_mfma_f32_32x32x16_bf16 v[208:223], v[72:75], v[160:163], v[208:223]\n"
        "ds_read_b64_tr_b16 v[60:61], v33 offset:30720\n"
        "ds_read_b64_tr_b16 v[62:63], v33 offset:31744\n"
        "s_waitcnt lgkmcnt(8)\n"
        "v_mfma_f32_32x32x16_bf16 v[224:239], v[76:79], v[160:163], v[224:239]\n"
        "ds_read_b64_tr_b16 v[64:65], v32 offset:33792\n"
        "ds_read_b64_tr_b16 v[66:67], v32 offset:34816\n"
        "s_waitcnt vmcnt(10)\n"
        "s_waitcnt lgkmcnt(8)\n"
        "v_mfma_f32_32x32x16_bf16 v[208:223], v[80:83], v[164:167], v[208:223]\n"
        "ds_read_b64_tr_b16 v[68:69], v33 offset:33792\n"
        "ds_read_b64_tr_b16 v[70:71], v33 offset:34816\n"
        "s_waitcnt lgkmcnt(8)\n"
        "v_mfma_f32_32x32x16_bf16 v[224:239], v[84:87], v[164:167], v[224:239]\n"
        "ds_read_b64_tr_b16 v[72:73], v32 offset:36864\n"
        "ds_read_b64_tr_b16 v[74:75], v32 offset:37888\n"
        "s_waitcnt vmcnt(9)\n"
        "s_waitcnt lgkmcnt(8)\n"
        "v_mfma_f32_32x32x16_bf16 v[208:223], v[56:59], v[168:171], v[208:223]\n"
        "ds_read_b64_tr_b16 v[76:77], v33 offset:36864\n"
        "ds_read_b64_tr_b16 v[78:79], v33 offset:37888\n"
        "s_waitcnt lgkmcnt(8)\n"
        "v_mfma_f32_32x32x16_bf16 v[224:239], v[60:63], v[168:171], v[224:239]\n"
        "ds_read_b64_tr_b16 v[80:81], v32 offset:39936\n"
        "ds_read_b64_tr_b16 v[82:83], v32 offset:40960\n"
        "s_waitcnt vmcnt(8)\n"
        "s_waitcnt lgkmcnt(8)\n"
        "v_mfma_f32_32x32x16_bf16 v[208:223], v[64:67], v[172:175], v[208:223]\n"
        "ds_read_b64_tr_b16 v[84:85], v33 offset:39936\n"
        "ds_read_b64_tr_b16 v[86:87], v33 offset:40960\n"
        "s_waitcnt lgkmcnt(8)\n"
        "v_mfma_f32_32x32x16_bf16 v[224:239], v[68:71], v[172:175], v[224:239]\n"
        "ds_read_b64_tr_b16 v[56:57], v32 offset:43008\n"
        "ds_read_b64_tr_b16 v[58:59], v32 offset:44032\n"
        "s_waitcnt vmcnt(7)\n"
        "s_waitcnt lgkmcnt(8)\n"
        "v_mfma_f32_32x32x16_bf16 v[208:223], v[72:75], v[176:179], v[208:223]\n"
        "ds_read_b64_tr_b16 v[60:61], v33 offset:43008\n"
        "ds_read_b64_tr_b16 v[62:63], v33 offset:44032\n"
        "s_waitcnt lgkmcnt(8)\n"
        "v_mfma_f32_32x32x16_bf16 v[224:239], v[76:79], v[176:179], v[224:239]\n"
        "ds_read_b64_tr_b16 v[64:65], v32 offset:46080\n"
        "ds_read_b64_tr_b16 v[66:67], v32 offset:47104\n"
        "s_waitcnt vmcnt(6)\n"
        "s_waitcnt lgkmcnt(8)\n"
        "v_mfma_f32_32x32x16_bf16 v[208:223], v[80:83], v[180:183], v[208:223]\n"
        "ds_read_b64_tr_b16 v[68:69], v33 offset:46080\n"
        "ds_read_b64_tr_b16 v[70:71], v33 offset:47104\n"
        "s_waitcnt lgkmcnt(8)\n"
        "v_mfma_f32_32x32x16_bf16 v[224:239], v[84:87], v[180:183], v[224:239]\n"
        "ds_read_b64_tr_b16 v[72:73], v35 offset:30720\n"
        "ds_read_b64_tr_b16 v[74:75], v35 offset:31744\n"
        "ds_read_b32 v20, v53\n"
        "s_waitcnt vmcnt(5)\n"
        "s_waitcnt lgkmcnt(9)\n"
        "v_mfma_f32_32x32x16_bf16 v[208:223], v[56:59], v[184:187], v[208:223]\n"
        "ds_read_b64_tr_b16 v[76:77], v36 offset:30720\n"
        "ds_read_b64_tr_b16 v[78:79], v36 offset:31744\n"
        "s_waitcnt lgkmcnt(9)\n"
        "v_mfma_f32_32x32x16_bf16 v[224:239], v[60:63], v[184:187], v[224:239]\n"
        "ds_read_b64_tr_b16 v[80:81], v38 offset:0\n"
        "ds_read_b64_tr_b16 v[82:83], v38 offset:1024\n"
        "s_waitcnt vmcnt(4)\n"
        "s_waitcnt lgkmcnt(9)\n"
        "v_mfma_f32_32x32x16_bf16 v[208:223], v[64:67], v[188:191], v[208:223]\n"
        "ds_read_b64_tr_b16 v[84:85], v38 offset:3072\n"
        "ds_read_b64_tr_b16 v[86:87], v38 offset:4096\n"
        "s_waitcnt lgkmcnt(6)\n"
        "v_cmp_gt_u32 vcc, 34, v20\n"
        "v_mfma_f32_32x32x16_bf16 v[224:239], v[68:71], v[188:191], v[224:239]\n"
        "s_cbranch_vccnz Lslow_90\n"
        "Lback_91:\n"
        "ds_read_b64_tr_b16 v[56:57], v38 offset:6144\n"
        "ds_read_b64_tr_b16 v[58:59], v38 offset:7168\n"
        "ds_read_b128 v[88:91], v47 offset:0\n"
        "ds_read_b128 v[92:95], v47 offset:1024\n"
        "s_waitcnt vmcnt(0)\n"
        "v_mfma_f32_32x32x16_bf16 v[208:223], v[72:75], v[192:195], v[208:223]\n"
        "ds_read_b64_tr_b16 v[60:61], v38 offset:9216\n"
        "ds_read_b64_tr_b16 v[62:63], v38 offset:10240\n"
        "ds_read_b128 v[96:99], v47 offset:2048\n"
        "ds_read_b128 v[100:103], v47 offset:3072\n"
        "s_waitcnt lgkmcnt(12)\n"
        "v_mfma_f32_32x32x16_bf16 v[224:239], v[76:79], v[192:195], v[224:239]\n"
        "ds_read_b64_tr_b16 v[64:65], v38 offset:12288\n"
        "ds_read_b64_tr_b16 v[66:67], v38 offset:13312\n"
        "ds_read_b128 v[104:107], v47 offset:4096\n"
        "ds_read_b128 v[108:111], v47 offset:5120\n"
        "s_waitcnt lgkmcnt(9)\n"
        "v_mfma_f32_32x32x16_bf16 v[208:223], v[80:83], v[88:91], v[208:223]\n"
        "ds_read_b64_tr_b16 v[68:69], v38 offset:15360\n"
        "ds_read_b64_tr_b16 v[70:71], v38 offset:16384\n"
        "ds_read_b128 v[112:115], v47 offset:6144\n"
        "ds_read_b128 v[116:119], v47 offset:7168\n"
        "s_waitcnt lgkmcnt(12)\n"
        "v_mfma_f32_32x32x16_bf16 v[208:223], v[84:87], v[92:95], v[208:223]\n"
        "ds_read_b64_tr_b16 v[72:73], v38 offset:18432\n"
        "ds_read_b64_tr_b16 v[74:75], v38 offset:19456\n"
        "ds_read_b32 v120, v25 offset:8192\n"
        "ds_read_b32 v121, v25 offset:8320\n"
        "s_waitcnt lgkmcnt(13)\n"
        "v_mfma_f32_32x32x16_bf16 v[208:223], v[56:59], v[96:99], v[208:223]\n"
        "ds_read_b64_tr_b16 v[76:77], v38 offset:21504\n"
        "ds_read_b64_tr_b16 v[78:79], v38 offset:22528\n"
        "ds_read_b32 v122, v25 offset:8448\n"
        "ds_read_b32 v123, v25 offset:8576\n"
        "s_waitcnt lgkmcnt(15)\n"
        "v_mfma_f32_32x32x16_bf16 v[208:223], v[60:63], v[100:103], v[208:223]\n"
        "ds_read_b64_tr_b16 v[80:81], v35 offset:33792\n"
        "ds_read_b64_tr_b16 v[82:83], v35 offset:34816\n"
        "s_waitcnt lgkmcnt(15)\n"
        "v_mfma_f32_32x32x16_bf16 v[208:223], v[64:67], v[104:107], v[208:223]\n"
        "ds_read_b64_tr_b16 v[84:85], v39 offset:0\n"
        "ds_read_b64_tr_b16 v[86:87], v39 offset:1024\n"
        "s_waitcnt lgkmcnt(14)\n"
        "v_mfma_f32_32x32x16_bf16 v[208:223], v[68:71], v[108:111], v[208:223]\n"
        "ds_read_b64_tr_b16 v[56:57], v39 offset:3072\n"
        "ds_read_b64_tr_b16 v[58:59], v39 offset:4096\n"
        "s_waitcnt lgkmcnt(12)\n"
        "v_mfma_f32_32x32x16_bf16 v[208:223], v[72:75], v[112:115], v[208:223]\n"
        "ds_read_b64_tr_b16 v[60:61], v39 offset:6144\n"
        "ds_read_b64_tr_b16 v[62:63], v39 offset:7168\n"
        "s_waitcnt lgkmcnt(10)\n"
        "v_mfma_f32_32x32x16_bf16 v[208:223], v[76:79], v[116:119], v[208:223]\n"
        "ds_read_b64_tr_b16 v[64:65], v39 offset:9216\n"
        "ds_read_b64_tr_b16 v[66:67], v39 offset:10240\n"
        "s_waitcnt lgkmcnt(8)\n"
        "v_mfma_f32_32x32x16_bf16 v[208:223], v[80:83], v[120:123], v[208:223]\n"
        "ds_read_b64_tr_b16 v[68:69], v39 offset:12288\n"
        "ds_read_b64_tr_b16 v[70:71], v39 offset:13312\n"
        "s_waitcnt lgkmcnt(8)\n"
        "v_mfma_f32_32x32x16_bf16 v[224:239], v[84:87], v[88:91], v[224:239]\n"
        "ds_read_b64_tr_b16 v[72:73], v39 offset:15360\n"
        "ds_read_b64_tr_b16 v[74:75], v39 offset:16384\n"
        "s_waitcnt lgkmcnt(8)\n"
        "v_mfma_f32_32x32x16_bf16 v[224:239], v[56:59], v[92:95], v[224:239]\n"
        "ds_read_b64_tr_b16 v[76:77], v39 offset:18432\n"
        "ds_read_b64_tr_b16 v[78:79], v39 offset:19456\n"
        "s_waitcnt lgkmcnt(8)\n"
        "v_mfma_f32_32x32x16_bf16 v[224:239], v[60:63], v[96:99], v[224:239]\n"
        "ds_read_b64_tr_b16 v[80:81], v39 offset:21504\n"
        "ds_read_b64_tr_b16 v[82:83], v39 offset:22528\n"
        "v_cvt_pk_bf16_f32 v124, v208, v209\n"
        "v_cvt_pk_bf16_f32 v125, v210, v211\n"
        "s_waitcnt lgkmcnt(8)\n"
        "v_mfma_f32_32x32x16_bf16 v[224:239], v[64:67], v[100:103], v[224:239]\n"
        "ds_read_b64_tr_b16 v[84:85], v36 offset:33792\n"
        "ds_read_b64_tr_b16 v[86:87], v36 offset:34816\n"
        "v_cvt_pk_bf16_f32 v126, v212, v213\n"
        "v_cvt_pk_bf16_f32 v127, v214, v215\n"
        "s_waitcnt lgkmcnt(8)\n"
        "v_mfma_f32_32x32x16_bf16 v[224:239], v[68:71], v[104:107], v[224:239]\n"
        "ds_write_b128 v41, v[124:127] offset:0\n"
        "v_cvt_pk_bf16_f32 v128, v216, v217\n"
        "s_waitcnt lgkmcnt(7)\n"
        "v_mfma_f32_32x32x16_bf16 v[224:239], v[72:75], v[108:111], v[224:239]\n"
        "v_cvt_pk_bf16_f32 v129, v218, v219\n"
        "v_cvt_pk_bf16_f32 v130, v220, v221\n"
        "s_waitcnt lgkmcnt(5)\n"
        "v_mfma_f32_32x32x16_bf16 v[224:239], v[76:79], v[112:115], v[224:239]\n"
        "v_cvt_pk_bf16_f32 v131, v222, v223\n"
        "ds_write_b128 v42, v[128:131] offset:0\n"
        "s_waitcnt lgkmcnt(4)\n"
        "v_mfma_f32_32x32x16_bf16 v[224:239], v[80:83], v[116:119], v[224:239]\n"
        "s_waitcnt lgkmcnt(2)\n"
        "v_mfma_f32_32x32x16_bf16 v[224:239], v[84:87], v[120:123], v[224:239]\n"
        "s_nop 11\n"
        "v_cvt_pk_bf16_f32 v132, v224, v225\n"
        "v_cvt_pk_bf16_f32 v133, v226, v227\n"
        "v_cvt_pk_bf16_f32 v134, v228, v229\n"
        "v_cvt_pk_bf16_f32 v135, v230, v231\n"
        "ds_write_b128 v43, v[132:135] offset:0\n"
        "v_cvt_pk_bf16_f32 v136, v232, v233\n"
        "v_cvt_pk_bf16_f32 v137, v234, v235\n"
        "v_cvt_pk_bf16_f32 v138, v236, v237\n"
        "v_cvt_pk_bf16_f32 v139, v238, v239\n"
        "ds_write_b128 v44, v[136:139] offset:0\n"
        "ds_read_b128 v[156:159], v47 offset:0\n"
        "ds_read_b128 v[160:163], v47 offset:1024\n"
        "ds_read_b128 v[164:167], v47 offset:2048\n"
        "ds_read_b128 v[168:171], v47 offset:3072\n"
        "s_mov_b32 s4, 0\n"
        "s_waitcnt lgkmcnt(3)\n"
        "buffer_store_dwordx4 v[156:159], v48, s[52:55], s4 offen sc1\n"
        "s_mov_b32 s4, 18432\n"
        "s_waitcnt lgkmcnt(2)\n"
        "buffer_store_dwordx4 v[160:163], v48, s[52:55], s4 offen sc1\n"
        "s_mov_b32 s4, 36864\n"
        "s_waitcnt lgkmcnt(1)\n"
        "buffer_store_dwordx4 v[164:167], v48, s[52:55], s4 offen sc1\n"
        "s_mov_b32 s4, 55296\n"
        "s_waitcnt lgkmcnt(0)\n"
        "buffer_store_dwordx4 v[168:171], v48, s[52:55], s4 offen sc1\n"
        "s_branch L_HDONE\n"
        "L_OP3:\n"
        "s_mov_b32 s4, 32768\n"
        "s_mov_b32 s7, 36864\n"
        "s_mov_b32 s57, 67584\n"
        "ds_read_b64_tr_b16 v[56:57], v35 offset:0\n"
        "ds_read_b64_tr_b16 v[58:59], v35 offset:1024\n"
        "ds_read_b64_tr_b16 v[60:61], v36 offset:0\n"
        "ds_read_b64_tr_b16 v[62:63], v36 offset:1024\n"
        "ds_read_b64_tr_b16 v[64:65], v37 offset:0\n"
        "ds_read_b64_tr_b16 v[66:67], v37 offset:512\n"
        "ds_read_b64_tr_b16 v[68:69], v35 offset:3072\n"
        "ds_read_b64_tr_b16 v[70:71], v35 offset:4096\n"
        "ds_read_b64_tr_b16 v[72:73], v36 offset:3072\n"
        "ds_read_b64_tr_b16 v[74:75], v36 offset:4096\n"
        "v_mov_b32 v22, 12\n"
        "s_waitcnt vmcnt(20)\n"
        "Lw_92:\n"
        "ds_read_b32 v20, v53\n"
        "s_waitcnt lgkmcnt(0)\n"
        "v_cmp_gt_u32 vcc, 31, v20\n"
        "s_cbranch_vccz Lwok_93\n"
        "s_sleep 1\n"
        "s_sub_u32 s56, s56, 1\n"
        "s_cbranch_scc1 L_ABORT\n"
        "s_branch Lw_92\n"
        "Lwok_93:\n"
        "v_mfma_f32_32x32x16_bf16 v[208:223], v[56:59], v[88:91], 0\n"
        "ds_read_b64_tr_b16 v[76:77], v37 offset:3072\n"
        "ds_read_b64_tr_b16 v[78:79], v37 offset:3584\n"
        "v_mfma_f32_32x32x16_bf16 v[224:239], v[60:63], v[88:91], 0\n"
        "ds_read_b64_tr_b16 v[80:81], v35 offset:6144\n"
        "ds_read_b64_tr_b16 v[82:83], v35 offset:7168\n"
        "v_mfma_f32_32x32x16_bf16 v[240:255], v[64:67], v[88:91], 0\n"
        "ds_read_b64_tr_b16 v[84:85], v36 offset:6144\n"
        "ds_read_b64_tr_b16 v[86:87], v36 offset:7168\n"
        "v_mfma_f32_32x32x16_bf16 v[208:223], v[68:71], v[92:95], v[208:223]\n"
        "ds_read_b64_tr_b16 v[56:57], v37 offset:6144\n"
        "ds_read_b64_tr_b16 v[58:59], v37 offset:6656\n"
        "v_mfma_f32_32x32x16_bf16 v[224:239], v[72:75], v[92:95], v[224:239]\n"
        "ds_read_b64_tr_b16 v[60:61], v35 offset:9216\n"
        "ds_read_b64_tr_b16 v[62:63], v35 offset:10240\n"
        "s_waitcnt lgkmcnt(8)\n"
        "v_mfma_f32_32x32x16_bf16 v[240:255], v[76:79], v[92:95], v[240:255]\n"
        "ds_read_b64_tr_b16 v[64:65], v36 offset:9216\n"
        "ds_read_b64_tr_b16 v[66:67], v36 offset:10240\n"
        "s_waitcnt lgkmcnt(8)\n"
        "v_mfma_f32_32x32x16_bf16 v[208:223], v[80:83], v[96:99], v[208:223]\n"
        "ds_read_b64_tr_b16 v[68:69], v37 offset:9216\n"
        "ds_read_b64_tr_b16 v[70:71], v37 offset:9728\n"
        "s_waitcnt lgkmcnt(8)\n"
        "v_mfma_f32_32x32x16_bf16 v[224:239], v[84:87], v[96:99], v[224:239]\n"
        "ds_read_b64_tr_b16 v[72:73], v35 offset:12288\n"
        "ds_read_b64_tr_b16 v[74:75], v35 offset:13312\n"
        "s_waitcnt lgkmcnt(8)\n"
        "v_mfma_f32_32x32x16_bf16 v[240:255], v[56:59], v[96:99], v[240:255]\n"
        "ds_read_b64_tr_b16 v[76:77], v36 offset:12288\n"
        "ds_read_b64_tr_b16 v[78:79], v36 offset:13312\n"
        "s_waitcnt lgkmcnt(8)\n"
        "v_mfma_f32_32x32x16_bf16 v[208:223], v[60:63], v[100:103], v[208:223]\n"
        "ds_read_b64_tr_b16 v[80:81], v37 offset:12288\n"
        "ds_read_b64_tr_b16 v[82:83], v37 offset:12800\n"
        "s_waitcnt lgkmcnt(8)\n"
        "v_mfma_f32_32x32x16_bf16 v[224:239], v[64:67], v[100:103], v[224:239]\n"
        "ds_read_b64_tr_b16 v[84:85], v35 offset:15360\n"
        "ds_read_b64_tr_b16 v[86:87], v35 offset:16384\n"
        "s_waitcnt lgkmcnt(8)\n"
        "v_mfma_f32_32x32x16_bf16 v[240:255], v[68:71], v[100:103], v[240:255]\n"
        "ds_read_b64_tr_b16 v[56:57], v36 offset:15360\n"
        "ds_read_b64_tr_b16 v[58:59], v36 offset:16384\n"
        "s_waitcnt lgkmcnt(8)\n"
        "v_mfma_f32_32x32x16_bf16 v[208:223], v[72:75], v[104:107], v[208:223]\n"
        "ds_read_b64_tr_b16 v[60:61], v37 offset:15360\n"
        "ds_read_b64_tr_b16 v[62:63], v37 offset:15872\n"
        "s_waitcnt lgkmcnt(8)\n"
        "v_mfma_f32_32x32x16_bf16 v[224:239], v[76:79], v[104:107], v[224:239]\n"
        "ds_read_b64_tr_b16 v[64:65], v35 offset:18432\n"
        "ds_read_b64_tr_b16 v[66:67], v35 offset:19456\n"
        "s_waitcnt lgkmcnt(8)\n"
        "v_mfma_f32_32x32x16_bf16 v[240:255], v[80:83], v[104:107], v[240:255]\n"
        "ds_read_b64_tr_b16 v[68:69], v36 offset:18432\n"
        "ds_read_b64_tr_b16 v[70:71], v36 offset:19456\n"
        "ds_read_b32 v20, v53\n"
        "s_waitcnt lgkmcnt(9)\n"
        "v_mfma_f32_32x32x16_bf16 v[208:223], v[84:87], v[108:111], v[208:223]\n"
        "ds_read_b64_tr_b16 v[72:73], v37 offset:18432\n"
        "ds_read_b64_tr_b16 v[74:75], v37 offset:18944\n"
        "s_waitcnt lgkmcnt(9)\n"
        "v_mfma_f32_32x32x16_bf16 v[224:239], v[56:59], v[108:111], v[224:239]\n"
        "ds_read_b64_tr_b16 v[76:77], v35 offset:21504\n"
        "ds_read_b64_tr_b16 v[78:79], v35 offset:22528\n"
        "s_waitcnt vmcnt(0)\n"
        "ds_write_b32 v18, v22\n"
        "s_waitcnt lgkmcnt(5)\n"
        "v_cmp_gt_u32 s[60:61], 33, v20\n"
        "v_mfma_f32_32x32x16_bf16 v[240:255], v[60:63], v[108:111], v[240:255]\n"
        "ds_read_b64_tr_b16 v[80:81], v36 offset:21504\n"
        "ds_read_b64_tr_b16 v[82:83], v36 offset:22528\n"
        "s_andn2_b32 s58, s68, s60\n"
        "s_cmp_eq_u32 s58, 0\n"
        "s_cbranch_scc1 Lskip_94\n"
        "buffer_load_dwordx4 v[160:163], v19, s[48:51], s4 offen offset:0 sc1\n"
        "Lskip_94:\n"
        "v_mfma_f32_32x32x16_bf16 v[208:223], v[64:67], v[112:115], v[208:223]\n"
        "ds_read_b64_tr_b16 v[84:85], v37 offset:21504\n"
        "ds_read_b64_tr_b16 v[86:87], v37 offset:22016\n"
        "s_cbranch_scc1 Lskip_95\n"
        "buffer_load_dwordx4 v[164:167], v19, s[48:51], s4 offen offset:1024 sc1\n"
        "Lskip_95:\n"
        "v_mfma_f32_32x32x16_bf16 v[224:239], v[68:71], v[112:115], v[224:239]\n"
        "ds_read_b64_tr_b16 v[56:57], v35 offset:24576\n"
        "ds_read_b64_tr_b16 v[58:59], v35 offset:25600\n"
        "s_cbranch_scc1 Lskip_96\n"
        "buffer_load_dwordx4 v[168:171], v19, s[48:51], s4 offen offset:2048 sc1\n"
        "Lskip_96:\n"
        "s_waitcnt lgkmcnt(9)\n"
        "v_mfma_f32_32x32x16_bf16 v[240:255], v[72:75], v[112:115], v[240:255]\n"
        "ds_read_b64_tr_b16 v[60:61], v36 offset:24576\n"
        "ds_read_b64_tr_b16 v[62:63], v36 offset:25600\n"
        "s_cbranch_scc1 Lskip_97\n"
        "buffer_load_dwordx4 v[172:175], v19, s[48:51], s4 offen offset:3072 sc1\n"
        "Lskip_97:\n"
        "s_waitcnt lgkmcnt(9)\n"
        "v_mfma_f32_32x32x16_bf16 v[208:223], v[76:79], v[116:119], v[208:223]\n"
        "ds_read_b64_tr_b16 v[64:65], v37 offset:24576\n"
        "ds_read_b64_tr_b16 v[66:67], v37 offset:25088\n"
        "s_cbranch_scc1 Lskip_98\n"
        "buffer_load_dwordx4 v[176:179], v19, s[48:51], s7 offen offset:0 sc1\n"
        "Lskip_98:\n"
        "s_waitcnt lgkmcnt(8)\n"
        "v_mfma_f32_32x32x16_bf16 v[224:239], v[80:83], v[116:119], v[224:239]\n"
        "ds_read_b64_tr_b16 v[68:69], v32 offset:0\n"
        "ds_read_b64_tr_b16 v[70:71], v32 offset:1024\n"
        "s_cbranch_scc1 Lskip_99\n"
        "buffer_load_dwordx4 v[180:183], v19, s[48:51], s7 offen offset:1024 sc1\n"
        "Lskip_99:\n"
        "s_waitcnt lgkmcnt(8)\n"
        "v_mfma_f32_32x32x16_bf16 v[240:255], v[84:87], v[116:119], v[240:255]\n"
        "ds_read_b64_tr_b16 v[72:73], v33 offset:0\n"
        "ds_read_b64_tr_b16 v[74:75], v33 offset:1024\n"
        "s_cbranch_scc1 Lskip_100\n"
        "buffer_load_dwordx4 v[184:187], v19, s[48:51], s7 offen offset:2048 sc1\n"
        "Lskip_100:\n"
        "s_waitcnt lgkmcnt(8)\n"
        "v_mfma_f32_32x32x16_bf16 v[208:223], v[56:59], v[120:123], v[208:223]\n"
        "ds_read_b64_tr_b16 v[76:77], v34 offset:0\n"
        "ds_read_b64_tr_b16 v[78:79], v34 offset:512\n"
        "s_cbranch_scc1 Lskip_101\n"
        "buffer_load_dwordx4 v[188:191], v19, s[48:51], s7 offen offset:3072 sc1\n"
        "Lskip_101:\n"
        "s_waitcnt lgkmcnt(8)\n"
        "v_mfma_f32_32x32x16_bf16 v[224:239], v[60:63], v[120:123], v[224:239]\n"
        "ds_read_b64_tr_b16 v[80:81], v32 offset:3072\n"
        "ds_read_b64_tr_b16 v[82:83], v32 offset:4096\n"
        "s_cbranch_scc1 Lskip_102\n"
        "buffer_load_dword v192, v15, s[48:51], s57 offen offset:0 sc1\n"
        "Lskip_102:\n"
        "s_waitcnt lgkmcnt(8)\n"
        "v_mfma_f32_32x32x16_bf16 v[240:255], v[64:67], v[120:123], v[240:255]\n"
        "ds_read_b64_tr_b16 v[84:85], v33 offset:3072\n"
        "ds_read_b64_tr_b16 v[86:87], v33 offset:4096\n"
        "s_cbranch_scc1 Lskip_103\n"
        "buffer_load_dword v193, v15, s[48:51], s57 offen offset:128 sc1\n"
        "Lskip_103:\n"
        "s_waitcnt lgkmcnt(8)\n"
        "v_mfma_f32_32x32x16_bf16 v[208:223], v[68:71], v[124:127], v[208:223]\n"
        "ds_read_b64_tr_b16 v[56:57], v34 offset:3072\n"
        "ds_read_b64_tr_b16 v[58:59], v34 offset:3584\n"
        "s_cbranch_scc1 Lskip_104\n"
        "buffer_load_dword v194, v15, s[48:51], s57 offen offset:256 sc1\n"
        "Lskip_104:\n"
        "s_waitcnt lgkmcnt(8)\n"
        "v_mfma_f32_32x32x16_bf16 v[224:239], v[72:75], v[124:127], v[224:239]\n"
        "ds_read_b64_tr_b16 v[60:61], v32 offset:6144\n"
        "ds_read_b64_tr_b16 v[62:63], v32 offset:7168\n"
        "s_cbranch_scc1 Lskip_105\n"
        "buffer_load_dword v195, v15, s[48:51], s57 offen offset:384 sc1\n"
        "Lskip_105:\n"
        "s_waitcnt lgkmcnt(8)\n"
        "v_mfma_f32_32x32x16_bf16 v[240:255], v[76:79], v[124:127], v[240:255]\n"
        "ds_read_b64_tr_b16 v[64:65], v33 offset:6144\n"
        "ds_read_b64_tr_b16 v[66:67], v33 offset:7168\n"
        "s_waitcnt lgkmcnt(8)\n"
        "v_mfma_f32_32x32x16_bf16 v[208:223], v[80:83], v[128:131], v[208:223]\n"
        "ds_read_b64_tr_b16 v[68:69], v34 offset:6144\n"
        "ds_read_b64_tr_b16 v[70:71], v34 offset:6656\n"
        "s_waitcnt lgkmcnt(8)\n"
        "v_mfma_f32_32x32x16_bf16 v[224:239], v[84:87], v[128:131], v[224:239]\n"
        "ds_read_b64_tr_b16 v[72:73], v32 offset:9216\n"
        "ds_read_b64_tr_b16 v[74:75], v32 offset:10240\n"
        "s_waitcnt lgkmcnt(8)\n"
        "v_mfma_f32_32x32x16_bf16 v[240:255], v[56:59], v[128:131], v[240:255]\n"
        "ds_read_b64_tr_b16 v[76:77], v33 offset:9216\n"
        "ds_read_b64_tr_b16 v[78:79], v33 offset:10240\n"
        "s_waitcnt lgkmcnt(8)\n"
        "v_mfma_f32_32x32x16_bf16 v[208:223], v[60:63], v[132:135], v[208:223]\n"
        "ds_read_b64_tr_b16 v[80:81], v34 offset:9216\n"
        "ds_read_b64_tr_b16 v[82:83], v34 offset:9728\n"
        "s_waitcnt lgkmcnt(8)\n"
        "v_mfma_f32_32x32x16_bf16 v[224:239], v[64:67], v[132:135], v[224:239]\n"
        "ds_read_b64_tr_b16 v[84:85], v32 offset:12288\n"
        "ds_read_b64_tr_b16 v[86:87], v32 offset:13312\n"
        "s_waitcnt lgkmcnt(8)\n"
        "v_mfma_f32_32x32x16_bf16 v[240:255], v[68:71], v[132:135], v[240:255]\n"
        "ds_read_b64_tr_b16 v[56:57], v33 offset:12288\n"
        "ds_read_b64_tr_b16 v[58:59], v33 offset:13312\n"
        "s_waitcnt lgkmcnt(8)\n"
        "v_mfma_f32_32x32x16_bf16 v[208:223], v[72:75], v[136:139], v[208:223]\n"
        "ds_read_b64_tr_b16 v[60:61], v34 offset:12288\n"
        "ds_read_b64_tr_b16 v[62:63], v34 offset:12800\n"
        "s_waitcnt lgkmcnt(8)\n"
        "v_mfma_f32_32x32x16_bf16 v[224:239], v[76:79], v[136:139], v[224:239]\n"
        "ds_read_b64_tr_b16 v[64:65], v32 offset:15360\n"
        "ds_read_b64_tr_b16 v[66:67], v32 offset:16384\n"
        "s_waitcnt lgkmcnt(8)\n"
        "v_mfma_f32_32x32x16_bf16 v[240:255], v[80:83], v[136:139], v[240:255]\n"
        "ds_read_b64_tr_b16 v[68:69], v33 offset:15360\n"
        "ds_read_b64_tr_b16 v[70:71], v33 offset:16384\n"
        "s_waitcnt lgkmcnt(8)\n"
        "v_mfma_f32_32x32x16_bf16 v[208:223], v[84:87], v[140:143], v[208:223]\n"
        "ds_read_b64_tr_b16 v[72:73], v34 offset:15360\n"
        "ds_read_b64_tr_b16 v[74:75], v34 offset:15872\n"
        "s_waitcnt lgkmcnt(8)\n"
        "v_mfma_f32_32x32x16_bf16 v[224:239], v[56:59], v[140:143], v[224:239]\n"
        "ds_read_b64_tr_b16 v[76:77], v32 offset:18432\n"
        "ds_read_b64_tr_b16 v[78:79], v32 offset:19456\n"
        "s_waitcnt lgkmcnt(8)\n"
        "v_mfma_f32_32x32x16_bf16 v[240:255], v[60:63], v[140:143], v[240:255]\n"
        "ds_read_b64_tr_b16 v[80:81], v33 offset:18432\n"
        "ds_read_b64_tr_b16 v[82:83], v33 offset:19456\n"
        "s_waitcnt lgkmcnt(8)\n"
        "v_mfma_f32_32x32x16_bf16 v[208:223], v[64:67], v[144:147], v[208:223]\n"
        "ds_read_b64_tr_b16 v[84:85], v34 offset:18432\n"
        "ds_read_b64_tr_b16 v[86:87], v34 offset:18944\n"
        "s_waitcnt lgkmcnt(8)\n"
        "v_mfma_f32_32x32x16_bf16 v[224:239], v[68:71], v[144:147], v[224:239]\n"
        "ds_read_b64_tr_b16 v[56:57], v32 offset:21504\n"
        "ds_read_b64_tr_b16 v[58:59], v32 offset:22528\n"
        "s_waitcnt lgkmcnt(8)\n"
        "v_mfma_f32_32x32x16_bf16 v[240:255], v[72:75], v[144:147], v[240:255]\n"
        "ds_read_b64_tr_b16 v[60:61], v33 offset:21504\n"
        "ds_read_b64_tr_b16 v[62:63], v33 offset:22528\n"
        "ds_read_b32 v20, v17\n"
        "s_waitcnt lgkmcnt(9)\n"
        "v_mfma_f32_32x32x16_bf16 v[208:223], v[76:79], v[148:151], v[208:223]\n"
        "ds_read_b64_tr_b16 v[64:65], v34 offset:21504\n"
        "ds_read_b64_tr_b16 v[66:67], v34 offset:22016\n"
        "s_waitcnt lgkmcnt(9)\n"
        "v_mfma_f32_32x32x16_bf16 v[224:239], v[80:83], v[148:151], v[224:239]\n"
        "ds_read_b64_tr_b16 v[68:69], v35 offset:27648\n"
        "ds_read_b64_tr_b16 v[70:71], v35 offset:28672\n"
        "s_waitcnt lgkmcnt(9)\n"
        "v_mfma_f32_32x32x16_bf16 v[240:255], v[84:87], v[148:151], v[240:255]\n"
        "ds_read_b64_tr_b16 v[72:73], v36 offset:27648\n"
        "ds_read_b64_tr_b16 v[74:75], v36 offset:28672\n"
        "s_waitcnt lgkmcnt(6)\n"
        "v_cmp_gt_u32 vcc, 12, v20\n"
        "v_mfma_f32_32x32x16_bf16 v[208:223], v[56:59], v[152:155], v[208:223]\n"
        "s_cbranch_vccnz Lslow_106\n"
        "Lback_107:\n"
        "ds_read_b64_tr_b16 v[76:77], v37 offset:27648\n"
        "ds_read_b64_tr_b16 v[78:79], v37 offset:28160\n"
        "v_mfma_f32_32x32x16_bf16 v[224:239], v[60:63], v[152:155], v[224:239]\n"
        "ds_read_b64_tr_b16 v[80:81], v32 offset:24576\n"
        "ds_read_b64_tr_b16 v[82:83], v32 offset:25600\n"
        "s_waitcnt lgkmcnt(8)\n"
        "v_mfma_f32_32x32x16_bf16 v[240:255], v[64:67], v[152:155], v[240:255]\n"
        "ds_read_b64_tr_b16 v[84:85], v33 offset:24576\n"
        "ds_read_b64_tr_b16 v[86:87], v33 offset:25600\n"
        "s_waitcnt lgkmcnt(8)\n"
        "v_mfma_f32_32x32x16_bf16 v[208:223], v[68:71], v[156:159], v[208:223]\n"
        "ds_read_b64_tr_b16 v[56:57], v34 offset:24576\n"
        "ds_read_b64_tr_b16 v[58:59], v34 offset:25088\n"
        "s_waitcnt lgkmcnt(8)\n"
        "v_mfma_f32_32x32x16_bf16 v[224:239], v[72:75], v[156:159], v[224:239]\n"
        "ds_read_b64_tr_b16 v[60:61], v32 offset:27648\n"
        "ds_read_b64_tr_b16 v[62:63], v32 offset:28672\n"
        "s_waitcnt lgkmcnt(8)\n"
        "v_mfma_f32_32x32x16_bf16 v[240:255], v[76:79], v[156:159], v[240:255]\n"
        "ds_read_b64_tr_b16 v[64:65], v33 offset:27648\n"
        "ds_read_b64_tr_b16 v[66:67], v33 offset:28672\n"
        "s_and_b32 s60, s60, s68\n"
        "s_cbranch_scc0 Lhave2_108\n"
        "Lw_109:\n"
        "ds_read_b32 v20, v53\n"
        "s_waitcnt lgkmcnt(0)\n"
        "v_cmp_gt_u32 vcc, 33, v20\n"
        "s_cbranch_vccz Lwok_110\n"
        "s_sleep 0\n"
        "s_sub_u32 s56, s56, 1\n"
        "s_cbranch_scc1 L_ABORT\n"
        "s_branch Lw_109\n"
        "Lwok_110:\n"
        "buffer_load_dwordx4 v[160:163], v19, s[48:51], s4 offen offset:0 sc1\n"
        "buffer_load_dwordx4 v[164:167], v19, s[48:51], s4 offen offset:1024 sc1\n"
        "buffer_load_dwordx4 v[168:171], v19, s[48:51], s4 offen offset:2048 sc1\n"
        "buffer_load_dwordx4 v[172:175], v19, s[48:51], s4 offen offset:3072 sc1\n"
        "buffer_load_dwordx4 v[176:179], v19, s[48:51], s7 offen offset:0 sc1\n"
        "buffer_load_dwordx4 v[180:183], v19, s[48:51], s7 offen offset:1024 sc1\n"
        "buffer_load_dwordx4 v[184:187], v19, s[48:51], s7 offen offset:2048 sc1\n"
        "buffer_load_dwordx4 v[188:191], v19, s[48:51], s7 offen offset:3072 sc1\n"
        "buffer_load_dword v192, v15, s[48:51], s57 offen offset:0 sc1\n"
        "buffer_load_dword v193, v15, s[48:51], s57 offen offset:128 sc1\n"
        "buffer_load_dword v194, v15, s[48:51], s57 offen offset:256 sc1\n"
        "buffer_load_dword v195, v15, s[48:51], s57 offen offset:384 sc1\n"
        "Lhave2_108:\n"
        "s_waitcnt vmcnt(11)\n"
        "s_waitcnt lgkmcnt(8)\n"
        "v_mfma_f32_32x32x16_bf16 v[208:223], v[80:83], v[160:163], v[208:223]\n"
        "ds_read_b64_tr_b16 v[68:69], v34 offset:27648\n"
        "ds_read_b64_tr_b16 v[70:71], v34 offset:28160\n"
        "s_waitcnt lgkmcnt(8)\n"
        "v_mfma_f32_32x32x16_bf16 v[224:239], v[84:87], v[160:163], v[224:239]\n"
        "ds_read_b64_tr_b16 v[72:73], v32 offset:30720\n"
        "ds_read_b64_tr_b16 v[74:75], v32 offset:31744\n"
        "s_waitcnt lgkmcnt(8)\n"
        "v_mfma_f32_32x32x16_bf16 v[240:255], v[56:59], v[160:163], v[240:255]\n"
        "ds_read_b64_tr_b16 v[76:77], v33 offset:30720\n"
        "ds_read_b64_tr_b16 v[78:79], v33 offset:31744\n"
        "s_waitcnt vmcnt(10)\n"
        "s_waitcnt lgkmcnt(8)\n"
        "v_mfma_f32_32x32x16_bf16 v[208:223], v[60:63], v[164:167], v[208:223]\n"
        "ds_read_b64_tr_b16 v[80:81], v34 offset:30720\n"
        "ds_read_b64_tr_b16 v[82:83], v34 offset:31232\n"
        "s_waitcnt lgkmcnt(8)\n"
        "v_mfma_f32_32x32x16_bf16 v[224:239], v[64:67], v[164:167], v[224:239]\n"
        "ds_read_b64_tr_b16 v[84:85], v32 offset:33792\n"
        "ds_read_b64_tr_b16 v[86:87], v32 offset:34816\n"
        "s_waitcnt lgkmcnt(8)\n"
        "v_mfma_f32_32x32x16_bf16 v[240:255], v[68:71], v[164:167], v[240:255]\n"
        "ds_read_b64_tr_b16 v[56:57], v33 offset:33792\n"
        "ds_read_b64_tr_b16 v[58:59], v33 offset:34816\n"
        "s_waitcnt vmcnt(9)\n"
        "s_waitcnt lgkmcnt(8)\n"
        "v_mfma_f32_32x32x16_bf16 v[208:223], v[72:75], v[168:171], v[208:223]\n"
        "ds_read_b64_tr_b16 v[60:61], v34 offset:33792\n"
        "ds_read_b64_tr_b16 v[62:63], v34 offset:34304\n"
        "s_waitcnt lgkmcnt(8)\n"
        "v_mfma_f32_32x32x16_bf16 v[224:239], v[76:79], v[168:171], v[224:239]\n"
        "ds_read_b64_tr_b16 v[64:65], v32 offset:36864\n"
        "ds_read_b64_tr_b16 v[66:67], v32 offset:37888\n"
        "s_waitcnt lgkmcnt(8)\n"
        "v_mfma_f32_32x32x16_bf16 v[240:255], v[80:83], v[168:171], v[240:255]\n"
        "ds_read_b64_tr_b16 v[68:69], v33 offset:36864\n"
        "ds_read_b64_tr_b16 v[70:71], v33 offset:37888\n"
        "s_waitcnt vmcnt(8)\n"
        "s_waitcnt lgkmcnt(8)\n"
        "v_mfma_f32_32x32x16_bf16 v[208:223], v[84:87], v[172:175], v[208:223]\n"
        "ds_read_b64_tr_b16 v[72:73], v34 offset:36864\n"
        "ds_read_b64_tr_b16 v[74:75], v34 offset:37376\n"
        "s_waitcnt lgkmcnt(8)\n"
        "v_mfma_f32_32x32x16_bf16 v[224:239], v[56:59], v[172:175], v[224:239]\n"
        "ds_read_b64_tr_b16 v[76:77], v32 offset:39936\n"
        "ds_read_b64_tr_b16 v[78:79], v32 offset:40960\n"
        "s_waitcnt lgkmcnt(8)\n"
        "v_mfma_f32_32x32x16_bf16 v[240:255], v[60:63], v[172:175], v[240:255]\n"
        "ds_read_b64_tr_b16 v[80:81], v33 offset:39936\n"
        "ds_read_b64_tr_b16 v[82:83], v33 offset:40960\n"
        "s_waitcnt vmcnt(7)\n"
        "s_waitcnt lgkmcnt(8)\n"
        "v_mfma_f32_32x32x16_bf16 v[208:223], v[64:67], v[176:179], v[208:223]\n"
        "ds_read_b64_tr_b16 v[84:85], v34 offset:39936\n"
        "ds_read_b64_tr_b16 v[86:87], v34 offset:40448\n"
        "s_waitcnt lgkmcnt(8)\n"
        "v_mfma_f32_32x32x16_bf16 v[224:239], v[68:71], v[176:179], v[224:239]\n"
        "ds_read_b64_tr_b16 v[56:57], v32 offset:43008\n"
        "ds_read_b64_tr_b16 v[58:59], v32 offset:44032\n"
        "s_waitcnt lgkmcnt(8)\n"
        "v_mfma_f32_32x32x16_bf16 v[240:255], v[72:75], v[176:179], v[240:255]\n"
        "ds_read_b64_tr_b16 v[60:61], v33 offset:43008\n"
        "ds_read_b64_tr_b16 v[62:63], v33 offset:44032\n"
        "s_waitcnt vmcnt(6)\n"
        "s_waitcnt lgkmcnt(8)\n"
        "v_mfma_f32_32x32x16_bf16 v[208:223], v[76:79], v[180:183], v[208:223]\n"
        "ds_read_b64_tr_b16 v[64:65], v34 offset:43008\n"
        "ds_read_b64_tr_b16 v[66:67], v34 offset:43520\n"
        "s_waitcnt lgkmcnt(8)\n"
        "v_mfma_f32_32x32x16_bf16 v[224:239], v[80:83], v[180:183], v[224:239]\n"
        "ds_read_b64_tr_b16 v[68:69], v32 offset:46080\n"
        "ds_read_b64_tr_b16 v[70:71], v32 offset:47104\n"
        "s_waitcnt lgkmcnt(8)\n"
        "v_mfma_f32_32x32x16_bf16 v[240:255], v[84:87], v[180:183], v[240:255]\n"
        "ds_read_b64_tr_b16 v[72:73], v33 offset:46080\n"
        "ds_read_b64_tr_b16 v[74:75], v33 offset:47104\n"
        "s_waitcnt vmcnt(5)\n"
        "s_waitcnt lgkmcnt(8)\n"
        "v_mfma_f32_32x32x16_bf16 v[208:223], v[56:59], v[184:187], v[208:223]\n"
        "ds_read_b64_tr_b16 v[76:77], v34 offset:46080\n"
        "ds_read_b64_tr_b16 v[78:79], v34 offset:46592\n"
        "s_waitcnt lgkmcnt(8)\n"
        "v_mfma_f32_32x32x16_bf16 v[224:239], v[60:63], v[184:187], v[224:239]\n"
        "ds_read_b64_tr_b16 v[80:81], v35 offset:30720\n"
        "ds_read_b64_tr_b16 v[82:83], v35 offset:31744\n"
        "s_waitcnt lgkmcnt(8)\n"
        "v_mfma_f32_32x32x16_bf16 v[240:255], v[64:67], v[184:187], v[240:255]\n"
        "ds_read_b64_tr_b16 v[84:85], v36 offset:30720\n"
        "ds_read_b64_tr_b16 v[86:87], v36 offset:31744\n"
        "ds_read_b32 v20, v53\n"
        "s_waitcnt vmcnt(4)\n"
        "s_waitcnt lgkmcnt(9)\n"
        "v_mfma_f32_32x32x16_bf16 v[208:223], v[68:71], v[188:191], v[208:223]\n"
        "ds_read_b64_tr_b16 v[56:57], v37 offset:30720\n"
        "ds_read_b64_tr_b16 v[58:59], v37 offset:31232\n"
        "s_waitcnt lgkmcnt(9)\n"
        "v_mfma_f32_32x32x16_bf16 v[224:239], v[72:75], v[188:191], v[224:239]\n"
        "ds_read_b64_tr_b16 v[60:61], v40 offset:0\n"
        "ds_read_b64_tr_b16 v[62:63], v40 offset:512\n"
        "s_waitcnt lgkmcnt(9)\n"
        "v_mfma_f32_32x32x16_bf16 v[240:255], v[76:79], v[188:191], v[240:255]\n"
        "ds_read_b64_tr_b16 v[64:65], v40 offset:3072\n"
        "ds_read_b64_tr_b16 v[66:67], v40 offset:3584\n"
        "s_waitcnt vmcnt(0)\n"
        "s_waitcnt lgkmcnt(6)\n"
        "v_cmp_gt_u32 vcc, 34, v20\n"
        "v_mfma_f32_32x32x16_bf16 v[208:223], v[80:83], v[192:195], v[208:223]\n"
        "s_cbranch_vccnz Lslow_111\n"
        "Lback_112:\n"
        "ds_read_b64_tr_b16 v[68:69], v40 offset:6144\n"
        "ds_read_b64_tr_b16 v[70:71], v40 offset:6656\n"
        "ds_read_b128 v[88:91], v47 offset:0\n"
        "ds_read_b128 v[92:95], v47 offset:1024\n"
        "v_mfma_f32_32x32x16_bf16 v[224:239], v[84:87], v[192:195], v[224:239]\n"
        "ds_read_b64_tr_b16 v[72:73], v40 offset:9216\n"
        "ds_read_b64_tr_b16 v[74:75], v40 offset:9728\n"
        "ds_read_b128 v[96:99], v47 offset:2048\n"
        "ds_read_b128 v[100:103], v47 offset:3072\n"
        "s_waitcnt lgkmcnt(12)\n"
        "v_mfma_f32_32x32x16_bf16 v[240:255], v[56:59], v[192:195], v[240:255]\n"
        "ds_read_b64_tr_b16 v[76:77], v40 offset:12288\n"
        "ds_read_b64_tr_b16 v[78:79], v40 offset:12800\n"
        "ds_read_b128 v[104:107], v47 offset:4096\n"
        "ds_read_b128 v[108:111], v47 offset:5120\n"
        "s_waitcnt lgkmcnt(9)\n"
        "v_mfma_f32_32x32x16_bf16 v[240:255], v[60:63], v[88:91], v[240:255]\n"
        "ds_read_b64_tr_b16 v[80:81], v40 offset:15360\n"
        "ds_read_b64_tr_b16 v[82:83], v40 offset:15872\n"
        "ds_read_b128 v[112:115], v47 offset:6144\n"
        "ds_read_b128 v[116:119], v47 offset:7168\n"
        "s_waitcnt lgkmcnt(12)\n"
        "v_mfma_f32_32x32x16_bf16 v[240:255], v[64:67], v[92:95], v[240:255]\n"
        "ds_read_b64_tr_b16 v[84:85], v40 offset:18432\n"
        "ds_read_b64_tr_b16 v[86:87], v40 offset:18944\n"
        "ds_read_b32 v120, v25 offset:8192\n"
        "ds_read_b32 v121, v25 offset:8320\n"
        "s_waitcnt lgkmcnt(13)\n"
        "v_mfma_f32_32x32x16_bf16 v[240:255], v[68:71], v[96:99], v[240:255]\n"
        "ds_read_b64_tr_b16 v[56:57], v40 offset:21504\n"
        "ds_read_b64_tr_b16 v[58:59], v40 offset:22016\n"
        "ds_read_b32 v122, v25 offset:8448\n"
        "ds_read_b32 v123, v25 offset:8576\n"
        "s_waitcnt lgkmcnt(15)\n"
        "v_mfma_f32_32x32x16_bf16 v[240:255], v[72:75], v[100:103], v[240:255]\n"
        "ds_read_b64_tr_b16 v[60:61], v37 offset:33792\n"
        "ds_read_b64_tr_b16 v[62:63], v37 offset:34304\n"
        "s_waitcnt lgkmcnt(15)\n"
        "v_mfma_f32_32x32x16_bf16 v[240:255], v[76:79], v[104:107], v[240:255]\n"
        "ds_read_b64_tr_b16 v[64:65], v38 offset:0\n"
        "ds_read_b64_tr_b16 v[66:67], v38 offset:1024\n"
        "s_waitcnt lgkmcnt(14)\n"
        "v_mfma_f32_32x32x16_bf16 v[240:255], v[80:83], v[108:111], v[240:255]\n"
        "ds_read_b64_tr_b16 v[68:69], v38 offset:3072\n"
        "ds_read_b64_tr_b16 v[70:71], v38 offset:4096\n"
        "s_waitcnt lgkmcnt(12)\n"
        "v_mfma_f32_32x32x16_bf16 v[240:255], v[84:87], v[112:115], v[240:255]\n"
        "ds_read_b64_tr_b16 v[72:73], v38 offset:6144\n"
        "ds_read_b64_tr_b16 v[74:75], v38 offset:7168\n"
        "s_waitcnt lgkmcnt(10)\n"
        "v_mfma_f32_32x32x16_bf16 v[240:255], v[56:59], v[116:119], v[240:255]\n"
        "ds_read_b64_tr_b16 v[76:77], v38 offset:9216\n"
        "ds_read_b64_tr_b16 v[78:79], v38 offset:10240\n"
        "s_waitcnt lgkmcnt(8)\n"
        "v_mfma_f32_32x32x16_bf16 v[240:255], v[60:63], v[120:123], v[240:255]\n"
        "ds_read_b64_tr_b16 v[80:81], v38 offset:12288\n"
        "ds_read_b64_tr_b16 v[82:83], v38 offset:13312\n"
        "s_waitcnt lgkmcnt(8)\n"
        "v_mfma_f32_32x32x16_bf16 v[208:223], v[64:67], v[88:91], v[208:223]\n"
        "ds_read_b64_tr_b16 v[84:85], v38 offset:15360\n"
        "ds_read_b64_tr_b16 v[86:87], v38 offset:16384\n"
        "s_waitcnt lgkmcnt(8)\n"
        "v_mfma_f32_32x32x16_bf16 v[208:223], v[68:71], v[92:95], v[208:223]\n"
        "ds_read_b64_tr_b16 v[56:57], v38 offset:18432\n"
        "ds_read_b64_tr_b16 v[58:59], v38 offset:19456\n"
        "s_waitcnt lgkmcnt(8)\n"
        "v_mfma_f32_32x32x16_bf16 v[208:223], v[72:75], v[96:99], v[208:223]\n"
        "ds_read_b64_tr_b16 v[60:61], v38 offset:21504\n"
        "ds_read_b64_tr_b16 v[62:63], v38 offset:22528\n"
        "v_cvt_pk_bf16_f32 v140, v240, v241\n"
        "v_cvt_pk_bf16_f32 v141, v242, v243\n"
        "s_waitcnt lgkmcnt(8)\n"
        "v_mfma_f32_32x32x16_bf16 v[208:223], v[76:79], v[100:103], v[208:223]\n"
        "ds_read_b64_tr_b16 v[64:65], v35 offset:33792\n"
        "ds_read_b64_tr_b16 v[66:67], v35 offset:34816\n"
        "v_cvt_pk_bf16_f32 v142, v244, v245\n"
        "v_cvt_pk_bf16_f32 v143, v246, v247\n"
        "s_waitcnt lgkmcnt(8)\n"
        "v_mfma_f32_32x32x16_bf16 v[208:223], v[80:83], v[104:107], v[208:223]\n"
        "ds_read_b64_tr_b16 v[68:69], v39 offset:0\n"
        "ds_read_b64_tr_b16 v[70:71], v39 offset:1024\n"
        "ds_write_b128 v45, v[140:143] offset:4096\n"
        "v_cvt_pk_bf16_f32 v144, v248, v249\n"
        "s_waitcnt lgkmcnt(9)\n"
        "v_mfma_f32_32x32x16_bf16 v[208:223], v[84:87], v[108:111], v[208:223]\n"
        "ds_read_b64_tr_b16 v[72:73], v39 offset:3072\n"
        "ds_read_b64_tr_b16 v[74:75], v39 offset:4096\n"
        "v_cvt_pk_bf16_f32 v145, v250, v251\n"
        "v_cvt_pk_bf16_f32 v146, v252, v253\n"
        "s_waitcnt lgkmcnt(9)\n"
        "v_mfma_f32_32x32x16_bf16 v[208:223], v[56:59], v[112:115], v[208:223]\n"
        "ds_read_b64_tr_b16 v[76:77], v39 offset:6144\n"
        "ds_read_b64_tr_b16 v[78:79], v39 offset:7168\n"
        "v_cvt_pk_bf16_f32 v147, v254, v255\n"
        "ds_write_b128 v46, v[144:147] offset:4096\n"
        "s_waitcnt lgkmcnt(10)\n"
        "v_mfma_f32_32x32x16_bf16 v[208:223], v[60:63], v[116:119], v[208:223]\n"
        "ds_read_b64_tr_b16 v[80:81], v39 offset:9216\n"
        "ds_read_b64_tr_b16 v[82:83], v39 offset:10240\n"
        "ds_read_b128 v[172:175], v47 offset:4096\n"
        "ds_read_b128 v[176:179], v47 offset:5120\n"
        "s_waitcnt lgkmcnt(12)\n"
        "v_mfma_f32_32x32x16_bf16 v[208:223], v[64:67], v[120:123], v[208:223]\n"
        "ds_read_b64_tr_b16 v[84:85], v39 offset:12288\n"
        "ds_read_b64_tr_b16 v[86:87], v39 offset:13312\n"
        "s_mov_b32 s4, 0\n"
        "s_waitcnt lgkmcnt(3)\n"
        "buffer_store_dwordx4 v[172:175], v49, s[52:55], s4 offen sc1\n"
        "s_mov_b32 s4, 36864\n"
        "s_waitcnt lgkmcnt(2)\n"
        "buffer_store_dwordx4 v[176:179], v49, s[52:55], s4 offen sc1\n"
        "v_mfma_f32_32x32x16_bf16 v[224:239], v[68:71], v[88:91], v[224:239]\n"
        "ds_read_b64_tr_b16 v[56:57], v39 offset:15360\n"
        "ds_read_b64_tr_b16 v[58:59], v39 offset:16384\n"
        "v_mfma_f32_32x32x16_bf16 v[224:239], v[72:75], v[92:95], v[224:239]\n"
        "ds_read_b64_tr_b16 v[60:61], v39 offset:18432\n"
        "ds_read_b64_tr_b16 v[62:63], v39 offset:19456\n"
        "v_mfma_f32_32x32x16_bf16 v[224:239], v[76:79], v[96:99], v[224:239]\n"
        "ds_read_b64_tr_b16 v[64:65], v39 offset:21504\n"
        "ds_read_b64_tr_b16 v[66:67], v39 offset:22528\n"
        "v_cvt_pk_bf16_f32 v124, v208, v209\n"
        "v_cvt_pk_bf16_f32 v125, v210, v211\n"
        "v_mfma_f32_32x32x16_bf16 v[224:239], v[80:83], v[100:103], v[224:239]\n"
        "ds_read_b64_tr_b16 v[68:69], v36 offset:33792\n"
        "ds_read_b64_tr_b16 v[70:71], v36 offset:34816\n"
        "v_cvt_pk_bf16_f32 v126, v212, v213\n"
        "v_cvt_pk_bf16_f32 v127, v214, v215\n"
        "s_waitcnt lgkmcnt(8)\n"
        "v_mfma_f32_32x32x16_bf16 v[224:239], v[84:87], v[104:107], v[224:239]\n"
        "ds_write_b128 v41, v[124:127] offset:0\n"
        "v_cvt_pk_bf16_f32 v128, v216, v217\n"
        "s_waitcnt lgkmcnt(7)\n"
        "v_mfma_f32_32x32x16_bf16 v[224:239], v[56:59], v[108:111], v[224:239]\n"
        "v_cvt_pk_bf16_f32 v129, v218, v219\n"
        "v_cvt_pk_bf16_f32 v130, v220, v221\n"
        "s_waitcnt lgkmcnt(5)\n"
        "v_mfma_f32_32x32x16_bf16 v[224:239], v[60:63], v[112:115], v[224:239]\n"
        "v_cvt_pk_bf16_f32 v131, v222, v223\n"
        "ds_write_b128 v42, v[128:131] offset:0\n"
        "s_waitcnt lgkmcnt(4)\n"
        "v_mfma_f32_32x32x16_bf16 v[224:239], v[64:67], v[116:119], v[224:239]\n"
        "s_waitcnt lgkmcnt(2)\n"
        "v_mfma_f32_32x32x16_bf16 v[224:239], v[68:71], v[120:123], v[224:239]\n"
        "s_nop 11\n"
        "v_cvt_pk_bf16_f32 v132, v224, v225\n"
        "v_cvt_pk_bf16_f32 v133, v226, v227\n"
        "v_cvt_pk_bf16_f32 v134, v228, v229\n"
        "v_cvt_pk_bf16_f32 v135, v230, v231\n"
        "ds_write_b128 v43, v[132:135] offset:0\n"
        "v_cvt_pk_bf16_f32 v136, v232, v233\n"
        "v_cvt_pk_bf16_f32 v137, v234, v235\n"
        "v_cvt_pk_bf16_f32 v138, v236, v237\n"
        "v_cvt_pk_bf16_f32 v139, v238, v239\n"
        "ds_write_b128 v44, v[136:139] offset:0\n"
        "ds_read_b128 v[156:159], v47 offset:0\n"
        "ds_read_b128 v[160:163], v47 offset:1024\n"
        "ds_read_b128 v[164:167], v47 offset:2048\n"
        "ds_read_b128 v[168:171], v47 offset:3072\n"
        "s_mov_b32 s4, 0\n"
        "s_waitcnt lgkmcnt(3)\n"
        "buffer_store_dwordx4 v[156:159], v48, s[52:55], s4 offen sc1\n"
        "s_mov_b32 s4, 18432\n"
        "s_waitcnt lgkmcnt(2)\n"
        "buffer_store_dwordx4 v[160:163], v48, s[52:55], s4 offen sc1\n"
        "s_mov_b32 s4, 36864\n"
        "s_waitcnt lgkmcnt(1)\n"
        "buffer_store_dwordx4 v[164:167], v48, s[52:55], s4 offen sc1\n"
        "s_mov_b32 s4, 55296\n"
        "s_waitcnt lgkmcnt(0)\n"
        "buffer_store_dwordx4 v[168:171], v48, s[52:55], s4 offen sc1\n"
        "L_HDONE:\n"
        "L_ABORT:\n"
        "s_endpgm\n"
        "Lpws_2:\n"
        "s_sub_u32 s56, s56, 1\n"
        "s_cbranch_scc1 L_ABORT\n"
        "v_mov_b32 v20, 128\n"
        "global_load_dwordx2 v[20:21], v20, s[16:17] sc1\n"
        "s_waitcnt vmcnt(0)\n"
        "v_readfirstlane_b32 s8, v20\n"
        "v_readfirstlane_b32 s9, v21\n"
        "s_nop 3\n"
        "s_branch Lpw_1\n"
        "Lvl_7:\n"
        "s_call_b64 s[64:65], L_VP8\n"
        "s_branch Lspm_6\n"
        "Lvl_14:\n"
        "s_call_b64 s[64:65], L_VP8\n"
        "s_branch Lspm_13\n"
        "Lvl_22:\n"
        "s_call_b64 s[64:65], L_VP8\n"
        "s_branch Lspm_21\n"
        "Lvl_27:\n"
        "s_call_b64 s[64:65], L_VP8\n"
        "s_branch Lspm_26\n"
        "Lvl_32:\n"
        "s_call_b64 s[64:65], L_VP8\n"
        "s_branch Lspm_31\n"
        "Lpws_41:\n"
        "s_sub_u32 s56, s56, 1\n"
        "s_cbranch_scc1 L_ABORT\n"
        "v_mov_b32 v20, 128\n"
        "global_load_dwordx2 v[20:21], v20, s[16:17] sc1\n"
        "s_waitcnt vmcnt(0)\n"
        "v_readfirstlane_b32 s8, v20\n"
        "v_readfirstlane_b32 s9, v21\n"
        "s_nop 3\n"
        "s_branch Lpw_40\n"
        "Lvl_56:\n"
        "s_call_b64 s[64:65], L_VP24\n"
        "s_branch Lspm_55\n"
        "Lvl_66:\n"
        "s_call_b64 s[64:65], L_VP24\n"
        "s_branch Lspm_65\n"
        "Lslow_85:\n"
        "ds_read_b32 v20, v17\n"
        "s_sub_u32 s56, s56, 1\n"
        "s_cbranch_scc1 L_ABORT\n"
        "s_waitcnt lgkmcnt(0)\n"
        "v_cmp_gt_u32 vcc, 12, v20\n"
        "s_cbranch_vccnz Lslow_85\n"
        "s_branch Lback_86\n"
        "Lslow_90:\n"
        "ds_read_b32 v20, v53\n"
        "s_sub_u32 s56, s56, 1\n"
        "s_cbranch_scc1 L_ABORT\n"
        "s_waitcnt lgkmcnt(0)\n"
        "v_cmp_gt_u32 vcc, 34, v20\n"
        "s_cbranch_vccnz Lslow_90\n"
        "s_branch Lback_91\n"
        "Lslow_106:\n"
        "ds_read_b32 v20, v17\n"
        "s_sub_u32 s56, s56, 1\n"
        "s_cbranch_scc1 L_ABORT\n"
        "s_waitcnt lgkmcnt(0)\n"
        "v_cmp_gt_u32 vcc, 12, v20\n"
        "s_cbranch_vccnz Lslow_106\n"
        "s_branch Lback_107\n"
        "Lslow_111:\n"
        "ds_read_b32 v20, v53\n"
        "s_sub_u32 s56, s56, 1\n"
        "s_cbranch_scc1 L_ABORT\n"
        "s_waitcnt lgkmcnt(0)\n"
        "v_cmp_gt_u32 vcc, 34, v20\n"
        "s_cbranch_vccnz Lslow_111\n"
        "s_branch Lback_112\n"
        ".p2align 6\n"
        "L_COMPUTE:\n"
        "s_waitcnt lgkmcnt(0)\n"
        "s_load_dwordx2 s[22:23], s[16:17], 0x0\n"
        "s_setprio 2\n"
        "s_lshl_b32 s20, s3, 3\n"
        "s_add_u32 s20, s20, s19\n"
        "s_lshl_b32 s20, s20, 4\n"
        "s_add_u32 s20, s20, s18\n"
        "s_and_b32 s83, s20, 127\n"
        "s_lshl_b32 s83, s83, 17\n"
        "s_lshl_b32 s21, s18, 2\n"
        "s_add_u32 s21, s21, s6\n"
        "s_lshl_b32 s21, s21, 17\n"
        "s_lshr_b32 s4, s83, 21\n"
        "s_add_u32 s4, s4, s3\n"
        "s_add_u32 s4, s4, 0\n"
        "s_and_b32 s4, s4, 1\n"
        "s_lshl_b32 s4, s4, 13\n"
        "s_add_u32 s83, s83, s4\n"
        "s_add_u32 s83, s83, 4194304\n"
        "s_lshr_b32 s4, s21, 21\n"
        "s_add_u32 s4, s4, s3\n"
        "s_add_u32 s4, s4, 0\n"
        "s_and_b32 s4, s4, 1\n"
        "s_lshl_b32 s4, s4, 13\n"
        "s_add_u32 s21, s21, s4\n"
        "s_add_u32 s21, s21, 20971520\n"
        "v_lshrrev_b32 v1, 2, v0\n"
        "v_mul_u32_u24 v1, 288, v1\n"
        "v_add_u32 v1, 122880, v1\n"
        "v_and_b32 v2, 3, v0\n"
        "v_lshl_add_u32 v232, v2, 3, v1\n"
        "v_mul_u32_u24 v4, 72, v2\n"
        "v_add3_u32 v4, v1, v4, 32\n"
        "v_lshrrev_b32 v5, 1, v2\n"
        "v_lshl_add_u32 v233, v5, 20, v4\n"
        "v_subrev_u32 v234, 122880, v232\n"
        "v_subrev_u32 v235, 122880, v233\n"
        "v_mul_u32_u24 v2, 260, v2\n"
        "v_add_u32 v252, 160768, v2\n"
        "s_lshl_b32 s4, s6, 8\n"
        "s_add_u32 s4, s4, 159744\n"
        "v_lshl_add_u32 v251, v0, 2, s4\n"
        "s_lshl_b32 s4, s6, 12\n"
        "s_add_u32 s4, s4, 24576\n"
        "v_and_b32 v5, 15, v0\n"
        "v_lshl_add_u32 v1, v5, 7, s4\n"
        "v_lshrrev_b32 v2, 4, v0\n"
        "v_bfe_u32 v4, v0, 1, 3\n"
        "v_xor_b32 v4, v2, v4\n"
        "v_lshl_add_u32 v236, v4, 4, v1\n"
        "v_xor_b32 v237, 64, v236\n"
        "v_lshlrev_b32 v1, 3, v0\n"
        "s_lshl_b32 s59, s6, 11\n"
        "v_lshl_add_u32 v238, v0, 4, s59\n"
        "v_add_u32 v239, 49152, v238\n"
        "v_lshlrev_b32 v4, 4, v0\n"
        "v_add_u32 v242, 98304, v4\n"
        "s_and_b32 s7, s6, 1\n"
        "s_mul_i32 s4, s7, 1536\n"
        "s_sub_u32 s4, s59, s4\n"
        "v_add_u32 v17, s4, v1\n"
        "v_add_u32 v240, 98304, v17\n"
        "s_mul_i32 s4, s7, 3072\n"
        "s_add_u32 s4, s4, 512\n"
        "v_add_u32 v241, s4, v240\n"
        "v_and_b32 v6, 7, v0\n"
        "v_lshlrev_b32 v6, 7, v6\n"
        "v_lshrrev_b32 v7, 3, v0\n"
        "v_lshl_or_b32 v253, v7, 28, v6\n"
        "s_lshl_b32 s49, s19, 7\n"
        "s_add_u32 s49, s49, s21\n"
        "s_add_u32 s49, s49, 69632\n"
        "v_mov_b32 v246, 0x3f803f80\n"
        "v_and_b32 v6, 3, v0\n"
        "v_cmp_eq_u32 vcc, 0, v6\n"
        "s_nop 1\n"
        "v_cndmask_b32 v246, 0, v246, vcc\n"
        "v_lshrrev_b32 v6, 5, v0\n"
        "v_and_b32 v7, 31, v0\n"
        "v_lshlrev_b32 v1, 2, v7\n"
        "v_lshl_or_b32 v243, v6, 28, v1\n"
        "v_lshlrev_b32 v1, 4, v5\n"
        "v_lshl_add_u32 v1, v6, 8, v1\n"
        "s_add_u32 s4, s59, 99328\n"
        "v_add_u32 v1, s4, v1\n"
        "v_and_b32 v2, 1, v2\n"
        "v_xor_b32 v2, 1, v2\n"
        "v_mul_u32_u24 v2, 24576, v2\n"
        "v_mad_i32_i24 v247, v2, -4, v1\n"
        "s_mov_b32 s4, 0xeefc0000\n"
        "v_bfe_u32 v4, s4, v7, 1\n"
        "v_lshl_add_u32 v247, v4, 20, v247\n"
        "v_mad_i32_i24 v248, v2, -3, v1\n"
        "s_mov_b32 s4, 0xeef30000\n"
        "v_bfe_u32 v4, s4, v7, 1\n"
        "v_lshl_add_u32 v248, v4, 20, v248\n"
        "v_mad_i32_i24 v249, v2, -2, v1\n"
        "s_mov_b32 s4, 0xeecf0000\n"
        "v_bfe_u32 v4, s4, v7, 1\n"
        "v_lshl_add_u32 v249, v4, 20, v249\n"
        "v_mad_i32_i24 v250, v2, -1, v1\n"
        "s_mov_b32 s4, 0xee3f0000\n"
        "v_bfe_u32 v4, s4, v7, 1\n"
        "v_lshl_add_u32 v250, v4, 20, v250\n"
        "v_mad_i32_i24 v1, v6, -4, v7\n"
        "s_lshl_b32 s4, s6, 3\n"
        "v_subrev_u32 v1, s4, v1\n"
        "s_mov_b32 s57, 0xff000000\n"
        "v_subrev_u32 v8, 0, v1\n"
        "v_and_b32 v8, s57, v8\n"
        "v_xor_b32 v12, s57, v8\n"
        "v_subrev_u32 v9, 1, v1\n"
        "v_and_b32 v9, s57, v9\n"
        "v_xor_b32 v13, s57, v9\n"
        "v_subrev_u32 v10, 2, v1\n"
        "v_and_b32 v10, s57, v10\n"
        "v_xor_b32 v14, s57, v10\n"
        "v_subrev_u32 v11, 3, v1\n"
        "v_and_b32 v11, s57, v11\n"
        "v_xor_b32 v15, s57, v11\n"
        "s_lshl_b32 s4, s6, 12\n"
        "v_add_u32 v18, s4, v242\n"
        "ds_write_b128 v18, v[8:11] offset:2048\n"
        "s_add_u32 s4, s4, 18432\n"
        "s_lshl_b32 s7, s6, 10\n"
        "s_add_u32 s7, s7, 61440\n"
        "s_cmp_lt_u32 s6, 2\n"
        "s_cselect_b32 s4, s4, s7\n"
        "v_add_u32 v1, s4, v242\n"
        "ds_write_b128 v1, v[12:15]\n"
        "s_mov_b32 s44, 0xff00ff00\n"
        "s_mov_b32 s45, 0xff00ff00\n"
        "s_lshl_b32 s55, s19, 3\n"
        "s_add_u32 s55, s55, s21\n"
        "s_add_u32 s55, s55, 69632\n"
        "s_lshl_b32 s68, s19, 10\n"
        "s_lshl_b32 s72, s19, 7\n"
        "s_add_u32 s72, s72, s59\n"
        "s_add_u32 s72, s72, 65536\n"
        "s_add_u32 s69, s68, 16384\n"
        "s_add_u32 s73, s72, 1024\n"
        "s_add_u32 s70, s68, 32768\n"
        "s_add_u32 s74, s72, 2048\n"
        "s_add_u32 s71, s68, 49152\n"
        "s_add_u32 s75, s72, 3072\n"
        "s_sub_u32 s58, s21, s59\n"
        "s_mov_b32 s51, 65536\n"
        "s_add_u32 s28, s16, s58\n"
        "s_addc_u32 s29, s17, 0\n"
        "s_and_b32 s29, s29, 0xffff\n"
        "s_mov_b32 s30, 73728\n"
        "s_mov_b32 s31, 0x20000\n"
        "s_add_u32 s24, s16, s83\n"
        "s_addc_u32 s25, s17, 0\n"
        "s_and_b32 s25, s25, 0xffff\n"
        "s_mov_b32 s26, 73728\n"
        "s_mov_b32 s27, 0x20000\n"
        "s_cmp_eq_u32 s18, 15\n"
        "s_cselect_b32 s26, 0, s26\n"
        "s_add_u32 s30, s30, s59\n"
        "s_add_u32 s40, s16, 0\n"
        "s_addc_u32 s41, s17, 0\n"
        "s_and_b32 s41, s41, 0xffff\n"
        "s_mov_b32 s42, 30408704\n"
        "s_mov_b32 s43, 0x20000\n"
        "s_mov_b32 s60, 0x3eae1a93\n"
        "s_mov_b32 s61, 0x3eae1a93\n"
        "s_mov_b32 s62, 0x53800000\n"
        "s_lshl_b32 s53, s18, 2\n"
        "s_add_u32 s53, s53, s6\n"
        "s_sub_i32 s53, 8, s53\n"
        "s_max_i32 s53, s53, 0\n"
        "s_waitcnt vmcnt(0) lgkmcnt(0)\n"
        "s_add_u32 s22, s22, 0x7f4a7c18\n"
        "s_addc_u32 s23, s23, 0x9e3779b9\n"
        "s_and_b32 s22, s22, -16\n"
        "s_add_u32 s4, s22, 1\n"
        "v_mov_b32 v244, s4\n"
        "v_mov_b32 v245, s23\n"
        "s_barrier\n"
        "ds_read_b128 v[0:3], v236 offset:24576\n"
        "ds_read_b128 v[4:7], v236 offset:26624\n"
        "ds_read_b128 v[8:11], v237 offset:24576\n"
        "ds_read_b128 v[12:15], v237 offset:26624\n"
        "ds_read_b64_tr_b16 v[168:169], v232 offset:27648\n"
        "ds_read_b64_tr_b16 v[170:171], v232 offset:27792\n"
        "ds_read_b64_tr_b16 v[172:173], v232 offset:27688\n"
        "ds_read_b64_tr_b16 v[174:175], v232 offset:27832\n"
        "ds_read_b64_tr_b16 v[176:177], v232 offset:27720\n"
        "ds_read_b64_tr_b16 v[178:179], v232 offset:27864\n"
        "ds_read_b64_tr_b16 v[180:181], v232 offset:27760\n"
        "ds_read_b64_tr_b16 v[182:183], v232 offset:27904\n"
        "ds_read_b64_tr_b16 v[184:185], v233 offset:27648\n"
        "ds_read_b64_tr_b16 v[186:187], v233 offset:27792\n"
        "ds_read_b64_tr_b16 v[188:189], v232 offset:32256\n"
        "s_waitcnt lgkmcnt(7)\n"
        "v_mfma_f32_16x16x32_bf16 v[128:131], v[168:171], v[0:3], 0\n"
        "ds_read_b64_tr_b16 v[190:191], v232 offset:32400\n"
        "v_mfma_f32_16x16x32_bf16 v[132:135], v[168:171], v[4:7], 0\n"
        "ds_read_b64_tr_b16 v[168:169], v232 offset:32296\n"
        "v_mfma_f32_16x16x32_bf16 v[140:143], v[172:175], v[4:7], 0\n"
        "ds_read_b64_tr_b16 v[170:171], v232 offset:32440\n"
        "v_mfma_f32_16x16x32_bf16 v[136:139], v[172:175], v[0:3], 0\n"
        "ds_read_b64_tr_b16 v[172:173], v232 offset:32328\n"
        "s_waitcnt lgkmcnt(7)\n"
        "v_mfma_f32_16x16x32_bf16 v[144:147], v[176:179], v[0:3], 0\n"
        "ds_read_b64_tr_b16 v[174:175], v232 offset:32472\n"
        "v_mfma_f32_16x16x32_bf16 v[148:151], v[176:179], v[4:7], 0\n"
        "ds_read_b64_tr_b16 v[176:177], v232 offset:32368\n"
        "v_mfma_f32_16x16x32_bf16 v[156:159], v[180:183], v[4:7], 0\n"
        "ds_read_b64_tr_b16 v[178:179], v232 offset:32512\n"
        "v_mfma_f32_16x16x32_bf16 v[152:155], v[180:183], v[0:3], 0\n"
        "ds_read_b64_tr_b16 v[180:181], v233 offset:32256\n"
        "s_waitcnt lgkmcnt(7)\n"
        "v_mfma_f32_16x16x32_bf16 v[160:163], v[184:187], v[0:3], 0\n"
        "ds_read_b64_tr_b16 v[182:183], v233 offset:32400\n"
        "v_mfma_f32_16x16x32_bf16 v[164:167], v[184:187], v[4:7], 0\n"
        "s_barrier\n"
        "ds_read_b128 v[16:19], v236 offset:49152\n"
        "ds_read_b128 v[20:23], v236 offset:51200\n"
        "ds_read_b128 v[24:27], v237 offset:49152\n"
        "ds_read_b128 v[28:31], v237 offset:51200\n"
        "ds_read_b64_tr_b16 v[184:185], v232 offset:18432\n"
        "v_mfma_f32_16x16x32_bf16 v[128:131], v[188:191], v[8:11], v[128:131]\n"
        "ds_read_b64_tr_b16 v[186:187], v232 offset:18576\n"
        "v_mfma_f32_16x16x32_bf16 v[132:135], v[188:191], v[12:15], v[132:135]\n"
        "ds_read_b64_tr_b16 v[188:189], v232 offset:18472\n"
        "s_waitcnt lgkmcnt(11)\n"
        "v_mfma_f32_16x16x32_bf16 v[140:143], v[168:171], v[12:15], v[140:143]\n"
        "ds_read_b64_tr_b16 v[190:191], v232 offset:18616\n"
        "v_mfma_f32_16x16x32_bf16 v[136:139], v[168:171], v[8:11], v[136:139]\n"
        "ds_read_b64_tr_b16 v[168:169], v232 offset:18504\n"
        "v_mfma_f32_16x16x32_bf16 v[144:147], v[172:175], v[8:11], v[144:147]\n"
        "ds_read_b64_tr_b16 v[170:171], v232 offset:18648\n"
        "v_mfma_f32_16x16x32_bf16 v[148:151], v[172:175], v[12:15], v[148:151]\n"
        "ds_read_b64_tr_b16 v[172:173], v232 offset:18544\n"
        "s_waitcnt lgkmcnt(11)\n"
        "v_mfma_f32_16x16x32_bf16 v[156:159], v[176:179], v[12:15], v[156:159]\n"
        "ds_read_b64_tr_b16 v[174:175], v232 offset:18688\n"
        "v_mfma_f32_16x16x32_bf16 v[152:155], v[176:179], v[8:11], v[152:155]\n"
        "ds_read_b64_tr_b16 v[176:177], v233 offset:18432\n"
        "v_mfma_f32_16x16x32_bf16 v[160:163], v[180:183], v[8:11], v[160:163]\n"
        "ds_read_b64_tr_b16 v[178:179], v233 offset:18576\n"
        "v_mfma_f32_16x16x32_bf16 v[164:167], v[180:183], v[12:15], v[164:167]\n"
        "ds_read_b64_tr_b16 v[180:181], v232 offset:23040\n"
        "s_waitcnt lgkmcnt(7)\n"
        "v_mfma_f32_16x16x32_bf16 v[128:131], v[184:187], v[16:19], v[128:131]\n"
        "ds_read_b64_tr_b16 v[182:183], v232 offset:23184\n"
        "v_mfma_f32_16x16x32_bf16 v[132:135], v[184:187], v[20:23], v[132:135]\n"
        "ds_read_b64_tr_b16 v[184:185], v232 offset:23080\n"
        "v_mfma_f32_16x16x32_bf16 v[140:143], v[188:191], v[20:23], v[140:143]\n"
        "ds_read_b64_tr_b16 v[186:187], v232 offset:23224\n"
        "v_mfma_f32_16x16x32_bf16 v[136:139], v[188:191], v[16:19], v[136:139]\n"
        "ds_read_b64_tr_b16 v[188:189], v232 offset:23112\n"
        "s_waitcnt lgkmcnt(7)\n"
        "v_mfma_f32_16x16x32_bf16 v[144:147], v[168:171], v[16:19], v[144:147]\n"
        "ds_read_b64_tr_b16 v[190:191], v232 offset:23256\n"
        "v_mfma_f32_16x16x32_bf16 v[148:151], v[168:171], v[20:23], v[148:151]\n"
        "ds_read_b64_tr_b16 v[168:169], v232 offset:23152\n"
        "v_mfma_f32_16x16x32_bf16 v[156:159], v[172:175], v[20:23], v[156:159]\n"
        "ds_read_b64_tr_b16 v[170:171], v232 offset:23296\n"
        "v_mfma_f32_16x16x32_bf16 v[152:155], v[172:175], v[16:19], v[152:155]\n"
        "ds_read_b64_tr_b16 v[172:173], v233 offset:23040\n"
        "s_waitcnt lgkmcnt(7)\n"
        "v_mfma_f32_16x16x32_bf16 v[160:163], v[176:179], v[16:19], v[160:163]\n"
        "ds_read_b64_tr_b16 v[174:175], v233 offset:23184\n"
        "v_mfma_f32_16x16x32_bf16 v[164:167], v[176:179], v[20:23], v[164:167]\n"
        "s_barrier\n"
        "ds_read_b128 v[32:35], v236 offset:0\n"
        "ds_read_b128 v[36:39], v236 offset:2048\n"
        "ds_read_b128 v[40:43], v237 offset:0\n"
        "ds_read_b128 v[44:47], v237 offset:2048\n"
        "ds_read_b64_tr_b16 v[176:177], v232 offset:9216\n"
        "v_mfma_f32_16x16x32_bf16 v[128:131], v[180:183], v[24:27], v[128:131]\n"
        "ds_read_b64_tr_b16 v[178:179], v232 offset:9360\n"
        "v_mfma_f32_16x16x32_bf16 v[132:135], v[180:183], v[28:31], v[132:135]\n"
        "ds_read_b64_tr_b16 v[180:181], v232 offset:9256\n"
        "s_waitcnt lgkmcnt(11)\n"
        "v_mfma_f32_16x16x32_bf16 v[140:143], v[184:187], v[28:31], v[140:143]\n"
        "ds_read_b64_tr_b16 v[182:183], v232 offset:9400\n"
        "v_mfma_f32_16x16x32_bf16 v[136:139], v[184:187], v[24:27], v[136:139]\n"
        "ds_read_b64_tr_b16 v[184:185], v232 offset:9288\n"
        "v_mfma_f32_16x16x32_bf16 v[144:147], v[188:191], v[24:27], v[144:147]\n"
        "ds_read_b64_tr_b16 v[186:187], v232 offset:9432\n"
        "v_mfma_f32_16x16x32_bf16 v[148:151], v[188:191], v[28:31], v[148:151]\n"
        "ds_read_b64_tr_b16 v[188:189], v232 offset:9328\n"
        "s_waitcnt lgkmcnt(11)\n"
        "v_mfma_f32_16x16x32_bf16 v[156:159], v[168:171], v[28:31], v[156:159]\n"
        "ds_read_b64_tr_b16 v[190:191], v232 offset:9472\n"
        "v_mfma_f32_16x16x32_bf16 v[152:155], v[168:171], v[24:27], v[152:155]\n"
        "ds_read_b64_tr_b16 v[168:169], v233 offset:9216\n"
        "v_mfma_f32_16x16x32_bf16 v[160:163], v[172:175], v[24:27], v[160:163]\n"
        "ds_read_b64_tr_b16 v[170:171], v233 offset:9360\n"
        "v_mfma_f32_16x16x32_bf16 v[164:167], v[172:175], v[28:31], v[164:167]\n"
        "ds_read_b64_tr_b16 v[172:173], v232 offset:13824\n"
        "s_waitcnt lgkmcnt(7)\n"
        "v_mfma_f32_16x16x32_bf16 v[128:131], v[176:179], v[32:35], v[128:131]\n"
        "ds_read_b64_tr_b16 v[174:175], v232 offset:13968\n"
        "v_mfma_f32_16x16x32_bf16 v[132:135], v[176:179], v[36:39], v[132:135]\n"
        "ds_read_b64_tr_b16 v[176:177], v232 offset:13864\n"
        "v_mfma_f32_16x16x32_bf16 v[140:143], v[180:183], v[36:39], v[140:143]\n"
        "ds_read_b64_tr_b16 v[178:179], v232 offset:14008\n"
        "v_mfma_f32_16x16x32_bf16 v[136:139], v[180:183], v[32:35], v[136:139]\n"
        "ds_read_b64_tr_b16 v[180:181], v232 offset:13896\n"
        "s_waitcnt lgkmcnt(7)\n"
        "v_mfma_f32_16x16x32_bf16 v[144:147], v[184:187], v[32:35], v[144:147]\n"
        "ds_read_b64_tr_b16 v[182:183], v232 offset:14040\n"
        "v_mfma_f32_16x16x32_bf16 v[148:151], v[184:187], v[36:39], v[148:151]\n"
        "ds_read_b64_tr_b16 v[184:185], v232 offset:13936\n"
        "v_mfma_f32_16x16x32_bf16 v[156:159], v[188:191], v[36:39], v[156:159]\n"
        "ds_read_b64_tr_b16 v[186:187], v232 offset:14080\n"
        "v_mfma_f32_16x16x32_bf16 v[152:155], v[188:191], v[32:35], v[152:155]\n"
        "ds_read_b64_tr_b16 v[188:189], v233 offset:13824\n"
        "s_waitcnt lgkmcnt(7)\n"
        "v_mfma_f32_16x16x32_bf16 v[160:163], v[168:171], v[32:35], v[160:163]\n"
        "ds_read_b64_tr_b16 v[190:191], v233 offset:13968\n"
        "v_mfma_f32_16x16x32_bf16 v[164:167], v[168:171], v[36:39], v[164:167]\n"
        "s_barrier\n"
        "ds_read_b128 v[48:51], v236 offset:24576\n"
        "ds_read_b128 v[52:55], v236 offset:26624\n"
        "ds_read_b128 v[56:59], v237 offset:24576\n"
        "ds_read_b128 v[60:63], v237 offset:26624\n"
        "ds_read_b64_tr_b16 v[168:169], v232 offset:0\n"
        "v_mfma_f32_16x16x32_bf16 v[128:131], v[172:175], v[40:43], v[128:131]\n"
        "ds_read_b64_tr_b16 v[170:171], v232 offset:144\n"
        "v_mfma_f32_16x16x32_bf16 v[132:135], v[172:175], v[44:47], v[132:135]\n"
        "ds_read_b64_tr_b16 v[172:173], v232 offset:40\n"
        "s_waitcnt lgkmcnt(11)\n"
        "v_mfma_f32_16x16x32_bf16 v[140:143], v[176:179], v[44:47], v[140:143]\n"
        "ds_read_b64_tr_b16 v[174:175], v232 offset:184\n"
        "v_mfma_f32_16x16x32_bf16 v[136:139], v[176:179], v[40:43], v[136:139]\n"
        "ds_read_b64_tr_b16 v[176:177], v232 offset:72\n"
        "v_mfma_f32_16x16x32_bf16 v[144:147], v[180:183], v[40:43], v[144:147]\n"
        "ds_read_b64_tr_b16 v[178:179], v232 offset:216\n"
        "v_mfma_f32_16x16x32_bf16 v[148:151], v[180:183], v[44:47], v[148:151]\n"
        "ds_read_b64_tr_b16 v[180:181], v232 offset:112\n"
        "s_waitcnt lgkmcnt(11)\n"
        "v_mfma_f32_16x16x32_bf16 v[156:159], v[184:187], v[44:47], v[156:159]\n"
        "ds_read_b64_tr_b16 v[182:183], v232 offset:256\n"
        "v_mfma_f32_16x16x32_bf16 v[152:155], v[184:187], v[40:43], v[152:155]\n"
        "ds_read_b64_tr_b16 v[184:185], v233 offset:0\n"
        "v_mfma_f32_16x16x32_bf16 v[160:163], v[188:191], v[40:43], v[160:163]\n"
        "ds_read_b64_tr_b16 v[186:187], v233 offset:144\n"
        "v_mfma_f32_16x16x32_bf16 v[164:167], v[188:191], v[44:47], v[164:167]\n"
        "ds_read_b64_tr_b16 v[188:189], v232 offset:4608\n"
        "s_waitcnt lgkmcnt(7)\n"
        "v_mfma_f32_16x16x32_bf16 v[128:131], v[168:171], v[48:51], v[128:131]\n"
        "ds_read_b64_tr_b16 v[190:191], v232 offset:4752\n"
        "v_mfma_f32_16x16x32_bf16 v[132:135], v[168:171], v[52:55], v[132:135]\n"
        "ds_read_b64_tr_b16 v[168:169], v232 offset:4648\n"
        "v_mfma_f32_16x16x32_bf16 v[140:143], v[172:175], v[52:55], v[140:143]\n"
        "ds_read_b64_tr_b16 v[170:171], v232 offset:4792\n"
        "v_mfma_f32_16x16x32_bf16 v[136:139], v[172:175], v[48:51], v[136:139]\n"
        "ds_read_b64_tr_b16 v[172:173], v232 offset:4680\n"
        "s_waitcnt lgkmcnt(7)\n"
        "v_mfma_f32_16x16x32_bf16 v[144:147], v[176:179], v[48:51], v[144:147]\n"
        "ds_read_b64_tr_b16 v[174:175], v232 offset:4824\n"
        "v_mfma_f32_16x16x32_bf16 v[148:151], v[176:179], v[52:55], v[148:151]\n"
        "ds_read_b64_tr_b16 v[176:177], v232 offset:4720\n"
        "v_mfma_f32_16x16x32_bf16 v[156:159], v[180:183], v[52:55], v[156:159]\n"
        "ds_read_b64_tr_b16 v[178:179], v232 offset:4864\n"
        "v_mfma_f32_16x16x32_bf16 v[152:155], v[180:183], v[48:51], v[152:155]\n"
        "ds_read_b64_tr_b16 v[180:181], v233 offset:4608\n"
        "s_waitcnt lgkmcnt(7)\n"
        "v_mfma_f32_16x16x32_bf16 v[160:163], v[184:187], v[48:51], v[160:163]\n"
        "ds_read_b64_tr_b16 v[182:183], v233 offset:4752\n"
        "v_mfma_f32_16x16x32_bf16 v[164:167], v[184:187], v[52:55], v[164:167]\n"
        "s_barrier\n"
        "ds_read_b128 v[64:67], v236 offset:49152\n"
        "ds_read_b128 v[68:71], v236 offset:51200\n"
        "ds_read_b128 v[72:75], v237 offset:49152\n"
        "ds_read_b128 v[76:79], v237 offset:51200\n"
        "ds_read_b64_tr_b16 v[184:185], v234 offset:0\n"
        "v_mfma_f32_16x16x32_bf16 v[128:131], v[188:191], v[56:59], v[128:131]\n"
        "ds_read_b64_tr_b16 v[186:187], v234 offset:144\n"
        "v_mfma_f32_16x16x32_bf16 v[132:135], v[188:191], v[60:63], v[132:135]\n"
        "ds_read_b64_tr_b16 v[188:189], v234 offset:40\n"
        "s_waitcnt lgkmcnt(11)\n"
        "v_mfma_f32_16x16x32_bf16 v[140:143], v[168:171], v[60:63], v[140:143]\n"
        "ds_read_b64_tr_b16 v[190:191], v234 offset:184\n"
        "v_mfma_f32_16x16x32_bf16 v[136:139], v[168:171], v[56:59], v[136:139]\n"
        "ds_read_b64_tr_b16 v[168:169], v234 offset:72\n"
        "v_mfma_f32_16x16x32_bf16 v[144:147], v[172:175], v[56:59], v[144:147]\n"
        "ds_read_b64_tr_b16 v[170:171], v234 offset:216\n"
        "v_mfma_f32_16x16x32_bf16 v[148:151], v[172:175], v[60:63], v[148:151]\n"
        "ds_read_b64_tr_b16 v[172:173], v234 offset:112\n"
        "s_waitcnt lgkmcnt(11)\n"
        "v_mfma_f32_16x16x32_bf16 v[156:159], v[176:179], v[60:63], v[156:159]\n"
        "ds_read_b64_tr_b16 v[174:175], v234 offset:256\n"
        "v_mfma_f32_16x16x32_bf16 v[152:155], v[176:179], v[56:59], v[152:155]\n"
        "ds_read_b64_tr_b16 v[176:177], v235 offset:0\n"
        "v_mfma_f32_16x16x32_bf16 v[160:163], v[180:183], v[56:59], v[160:163]\n"
        "ds_read_b64_tr_b16 v[178:179], v235 offset:144\n"
        "v_mfma_f32_16x16x32_bf16 v[164:167], v[180:183], v[60:63], v[164:167]\n"
        "ds_read_b64_tr_b16 v[180:181], v234 offset:4608\n"
        "s_waitcnt lgkmcnt(7)\n"
        "v_mfma_f32_16x16x32_bf16 v[128:131], v[184:187], v[64:67], v[128:131]\n"
        "ds_read_b64_tr_b16 v[182:183], v234 offset:4752\n"
        "v_mfma_f32_16x16x32_bf16 v[132:135], v[184:187], v[68:71], v[132:135]\n"
        "ds_read_b64_tr_b16 v[184:185], v234 offset:4648\n"
        "v_mfma_f32_16x16x32_bf16 v[140:143], v[188:191], v[68:71], v[140:143]\n"
        "ds_read_b64_tr_b16 v[186:187], v234 offset:4792\n"
        "v_mfma_f32_16x16x32_bf16 v[136:139], v[188:191], v[64:67], v[136:139]\n"
        "ds_read_b64_tr_b16 v[188:189], v234 offset:4680\n"
        "s_waitcnt lgkmcnt(7)\n"
        "v_mfma_f32_16x16x32_bf16 v[144:147], v[168:171], v[64:67], v[144:147]\n"
        "ds_read_b64_tr_b16 v[190:191], v234 offset:4824\n"
        "v_mfma_f32_16x16x32_bf16 v[148:151], v[168:171], v[68:71], v[148:151]\n"
        "ds_read_b64_tr_b16 v[168:169], v234 offset:4720\n"
        "v_mfma_f32_16x16x32_bf16 v[156:159], v[172:175], v[68:71], v[156:159]\n"
        "ds_read_b64_tr_b16 v[170:171], v234 offset:4864\n"
        "v_mfma_f32_16x16x32_bf16 v[152:155], v[172:175], v[64:67], v[152:155]\n"
        "ds_read_b64_tr_b16 v[172:173], v235 offset:4608\n"
        "s_waitcnt lgkmcnt(7)\n"
        "v_mfma_f32_16x16x32_bf16 v[160:163], v[176:179], v[64:67], v[160:163]\n"
        "ds_read_b64_tr_b16 v[174:175], v235 offset:4752\n"
        "v_mfma_f32_16x16x32_bf16 v[164:167], v[176:179], v[68:71], v[164:167]\n"
        "s_barrier\n"
        "ds_read_b64_tr_b16 v[176:177], v232 offset:27648\n"
        "v_mfma_f32_16x16x32_bf16 v[128:131], v[180:183], v[72:75], v[128:131]\n"
        "ds_read_b64_tr_b16 v[178:179], v232 offset:27792\n"
        "v_mfma_f32_16x16x32_bf16 v[132:135], v[180:183], v[76:79], v[132:135]\n"
        "ds_read_b64_tr_b16 v[180:181], v232 offset:27688\n"
        "s_waitcnt lgkmcnt(7)\n"
        "v_mfma_f32_16x16x32_bf16 v[140:143], v[184:187], v[76:79], v[140:143]\n"
        "ds_read_b64_tr_b16 v[182:183], v232 offset:27832\n"
        "v_mfma_f32_16x16x32_bf16 v[136:139], v[184:187], v[72:75], v[136:139]\n"
        "ds_read_b64_tr_b16 v[184:185], v232 offset:27720\n"
        "v_mfma_f32_16x16x32_bf16 v[144:147], v[188:191], v[72:75], v[144:147]\n"
        "ds_read_b64_tr_b16 v[186:187], v232 offset:27864\n"
        "v_mfma_f32_16x16x32_bf16 v[148:151], v[188:191], v[76:79], v[148:151]\n"
        "ds_read_b64_tr_b16 v[188:189], v232 offset:27760\n"
        "s_waitcnt lgkmcnt(7)\n"
        "v_mfma_f32_16x16x32_bf16 v[156:159], v[168:171], v[76:79], v[156:159]\n"
        "ds_read_b64_tr_b16 v[190:191], v232 offset:27904\n"
        "v_mfma_f32_16x16x32_bf16 v[152:155], v[168:171], v[72:75], v[152:155]\n"
        "ds_read_b64_tr_b16 v[168:169], v233 offset:27648\n"
        "v_mfma_f32_16x16x32_bf16 v[160:163], v[172:175], v[72:75], v[160:163]\n"
        "ds_read_b64_tr_b16 v[170:171], v233 offset:27792\n"
        "v_mfma_f32_16x16x32_bf16 v[164:167], v[172:175], v[76:79], v[164:167]\n"
        "ds_read_b64_tr_b16 v[172:173], v232 offset:32256\n"
        "s_waitcnt lgkmcnt(7)\n"
        "v_mfma_f32_16x16x32_bf16 v[192:195], v[0:3], v[176:179], 0\n"
        "ds_read_b64_tr_b16 v[174:175], v232 offset:32400\n"
        "v_mfma_f32_16x16x32_bf16 v[196:199], v[4:7], v[176:179], 0\n"
        "ds_read_b64_tr_b16 v[176:177], v232 offset:32296\n"
        "v_mfma_f32_16x16x32_bf16 v[204:207], v[4:7], v[180:183], 0\n"
        "ds_read_b64_tr_b16 v[178:179], v232 offset:32440\n"
        "v_mfma_f32_16x16x32_bf16 v[200:203], v[0:3], v[180:183], 0\n"
        "ds_read_b64_tr_b16 v[180:181], v232 offset:32328\n"
        "s_waitcnt lgkmcnt(7)\n"
        "v_mfma_f32_16x16x32_bf16 v[208:211], v[0:3], v[184:187], 0\n"
        "ds_read_b64_tr_b16 v[182:183], v232 offset:32472\n"
        "v_mfma_f32_16x16x32_bf16 v[212:215], v[4:7], v[184:187], 0\n"
        "ds_read_b64_tr_b16 v[184:185], v232 offset:32368\n"
        "v_mfma_f32_16x16x32_bf16 v[220:223], v[4:7], v[188:191], 0\n"
        "ds_read_b64_tr_b16 v[186:187], v232 offset:32512\n"
        "v_mfma_f32_16x16x32_bf16 v[216:219], v[0:3], v[188:191], 0\n"
        "ds_read_b64_tr_b16 v[188:189], v233 offset:32256\n"
        "s_waitcnt lgkmcnt(7)\n"
        "v_mfma_f32_16x16x32_bf16 v[224:227], v[0:3], v[168:171], 0\n"
        "ds_read_b64_tr_b16 v[190:191], v233 offset:32400\n"
        "v_mfma_f32_16x16x32_bf16 v[228:231], v[4:7], v[168:171], 0\n"
        "s_barrier\n"
        "ds_read_b128 v[80:83], v236 offset:24576\n"
        "ds_read_b128 v[84:87], v236 offset:26624\n"
        "ds_read_b128 v[88:91], v237 offset:24576\n"
        "ds_read_b128 v[92:95], v237 offset:26624\n"
        "ds_read_b64_tr_b16 v[168:169], v232 offset:18432\n"
        "v_mfma_f32_16x16x32_bf16 v[192:195], v[8:11], v[172:175], v[192:195]\n"
        "ds_read_b64_tr_b16 v[170:171], v232 offset:18576\n"
        "v_mfma_f32_16x16x32_bf16 v[196:199], v[12:15], v[172:175], v[196:199]\n"
        "ds_read_b64_tr_b16 v[172:173], v232 offset:18472\n"
        "s_waitcnt lgkmcnt(11)\n"
        "v_mfma_f32_16x16x32_bf16 v[204:207], v[12:15], v[176:179], v[204:207]\n"
        "ds_read_b64_tr_b16 v[174:175], v232 offset:18616\n"
        "v_mfma_f32_16x16x32_bf16 v[200:203], v[8:11], v[176:179], v[200:203]\n"
        "ds_read_b64_tr_b16 v[176:177], v232 offset:18504\n"
        "v_mfma_f32_16x16x32_bf16 v[208:211], v[8:11], v[180:183], v[208:211]\n"
        "ds_read_b64_tr_b16 v[178:179], v232 offset:18648\n"
        "v_mfma_f32_16x16x32_bf16 v[212:215], v[12:15], v[180:183], v[212:215]\n"
        "ds_read_b64_tr_b16 v[180:181], v232 offset:18544\n"
        "s_waitcnt lgkmcnt(11)\n"
        "v_mfma_f32_16x16x32_bf16 v[220:223], v[12:15], v[184:187], v[220:223]\n"
        "ds_read_b64_tr_b16 v[182:183], v232 offset:18688\n"
        "v_mfma_f32_16x16x32_bf16 v[216:219], v[8:11], v[184:187], v[216:219]\n"
        "ds_read_b64_tr_b16 v[184:185], v233 offset:18432\n"
        "v_mfma_f32_16x16x32_bf16 v[224:227], v[8:11], v[188:191], v[224:227]\n"
        "ds_read_b64_tr_b16 v[186:187], v233 offset:18576\n"
        "v_mfma_f32_16x16x32_bf16 v[228:231], v[12:15], v[188:191], v[228:231]\n"
        "ds_read_b64_tr_b16 v[188:189], v232 offset:23040\n"
        "s_waitcnt lgkmcnt(7)\n"
        "v_mfma_f32_16x16x32_bf16 v[128:131], v[168:171], v[80:83], v[128:131]\n"
        "ds_read_b64_tr_b16 v[190:191], v232 offset:23184\n"
        "v_mfma_f32_16x16x32_bf16 v[132:135], v[168:171], v[84:87], v[132:135]\n"
        "ds_read_b64_tr_b16 v[168:169], v232 offset:23080\n"
        "v_mfma_f32_16x16x32_bf16 v[140:143], v[172:175], v[84:87], v[140:143]\n"
        "ds_read_b64_tr_b16 v[170:171], v232 offset:23224\n"
        "v_mfma_f32_16x16x32_bf16 v[136:139], v[172:175], v[80:83], v[136:139]\n"
        "ds_read_b64_tr_b16 v[172:173], v232 offset:23112\n"
        "s_waitcnt lgkmcnt(7)\n"
        "v_mfma_f32_16x16x32_bf16 v[144:147], v[176:179], v[80:83], v[144:147]\n"
        "ds_read_b64_tr_b16 v[174:175], v232 offset:23256\n"
        "v_mfma_f32_16x16x32_bf16 v[148:151], v[176:179], v[84:87], v[148:151]\n"
        "ds_read_b64_tr_b16 v[176:177], v232 offset:23152\n"
        "v_mfma_f32_16x16x32_bf16 v[156:159], v[180:183], v[84:87], v[156:159]\n"
        "ds_read_b64_tr_b16 v[178:179], v232 offset:23296\n"
        "v_mfma_f32_16x16x32_bf16 v[152:155], v[180:183], v[80:83], v[152:155]\n"
        "ds_read_b64_tr_b16 v[180:181], v233 offset:23040\n"
        "s_waitcnt lgkmcnt(7)\n"
        "v_mfma_f32_16x16x32_bf16 v[160:163], v[184:187], v[80:83], v[160:163]\n"
        "ds_read_b64_tr_b16 v[182:183], v233 offset:23184\n"
        "v_mfma_f32_16x16x32_bf16 v[164:167], v[184:187], v[84:87], v[164:167]\n"
        "s_barrier\n"
        "ds_read_b64_tr_b16 v[184:185], v232 offset:9216\n"
        "v_mfma_f32_16x16x32_bf16 v[128:131], v[188:191], v[88:91], v[128:131]\n"
        "ds_read_b64_tr_b16 v[186:187], v232 offset:9360\n"
        "v_mfma_f32_16x16x32_bf16 v[132:135], v[188:191], v[92:95], v[132:135]\n"
        "ds_read_b64_tr_b16 v[188:189], v232 offset:9256\n"
        "s_waitcnt lgkmcnt(7)\n"
        "v_mfma_f32_16x16x32_bf16 v[140:143], v[168:171], v[92:95], v[140:143]\n"
        "ds_read_b64_tr_b16 v[190:191], v232 offset:9400\n"
        "v_mfma_f32_16x16x32_bf16 v[136:139], v[168:171], v[88:91], v[136:139]\n"
        "ds_read_b64_tr_b16 v[168:169], v232 offset:9288\n"
        "v_mfma_f32_16x16x32_bf16 v[144:147], v[172:175], v[88:91], v[144:147]\n"
        "ds_read_b64_tr_b16 v[170:171], v232 offset:9432\n"
        "v_mfma_f32_16x16x32_bf16 v[148:151], v[172:175], v[92:95], v[148:151]\n"
        "ds_read_b64_tr_b16 v[172:173], v232 offset:9328\n"
        "s_waitcnt lgkmcnt(7)\n"
        "v_mfma_f32_16x16x32_bf16 v[156:159], v[176:179], v[92:95], v[156:159]\n"
        "ds_read_b64_tr_b16 v[174:175], v232 offset:9472\n"
        "v_mfma_f32_16x16x32_bf16 v[152:155], v[176:179], v[88:91], v[152:155]\n"
        "ds_read_b64_tr_b16 v[176:177], v233 offset:9216\n"
        "v_mfma_f32_16x16x32_bf16 v[160:163], v[180:183], v[88:91], v[160:163]\n"
        "ds_read_b64_tr_b16 v[178:179], v233 offset:9360\n"
        "v_mfma_f32_16x16x32_bf16 v[164:167], v[180:183], v[92:95], v[164:167]\n"
        "ds_read_b64_tr_b16 v[180:181], v232 offset:13824\n"
        "s_waitcnt lgkmcnt(7)\n"
        "v_mfma_f32_16x16x32_bf16 v[192:195], v[16:19], v[184:187], v[192:195]\n"
        "ds_read_b64_tr_b16 v[182:183], v232 offset:13968\n"
        "v_mfma_f32_16x16x32_bf16 v[196:199], v[20:23], v[184:187], v[196:199]\n"
        "ds_read_b64_tr_b16 v[184:185], v232 offset:13864\n"
        "v_mfma_f32_16x16x32_bf16 v[204:207], v[20:23], v[188:191], v[204:207]\n"
        "ds_read_b64_tr_b16 v[186:187], v232 offset:14008\n"
        "v_mfma_f32_16x16x32_bf16 v[200:203], v[16:19], v[188:191], v[200:203]\n"
        "ds_read_b64_tr_b16 v[188:189], v232 offset:13896\n"
        "s_waitcnt lgkmcnt(7)\n"
        "v_mfma_f32_16x16x32_bf16 v[208:211], v[16:19], v[168:171], v[208:211]\n"
        "ds_read_b64_tr_b16 v[190:191], v232 offset:14040\n"
        "v_mfma_f32_16x16x32_bf16 v[212:215], v[20:23], v[168:171], v[212:215]\n"
        "ds_read_b64_tr_b16 v[168:169], v232 offset:13936\n"
        "v_mfma_f32_16x16x32_bf16 v[220:223], v[20:23], v[172:175], v[220:223]\n"
        "ds_read_b64_tr_b16 v[170:171], v232 offset:14080\n"
        "v_mfma_f32_16x16x32_bf16 v[216:219], v[16:19], v[172:175], v[216:219]\n"
        "ds_read_b64_tr_b16 v[172:173], v233 offset:13824\n"
        "s_waitcnt lgkmcnt(7)\n"
        "v_mfma_f32_16x16x32_bf16 v[224:227], v[16:19], v[176:179], v[224:227]\n"
        "ds_read_b64_tr_b16 v[174:175], v233 offset:13968\n"
        "v_mfma_f32_16x16x32_bf16 v[228:231], v[20:23], v[176:179], v[228:231]\n"
        "s_barrier\n"
        "ds_read_b128 v[96:99], v236 offset:49152\n"
        "ds_read_b128 v[100:103], v236 offset:51200\n"
        "ds_read_b128 v[104:107], v237 offset:49152\n"
        "ds_read_b128 v[108:111], v237 offset:51200\n"
        "ds_read_b64_tr_b16 v[176:177], v232 offset:0\n"
        "v_mfma_f32_16x16x32_bf16 v[192:195], v[24:27], v[180:183], v[192:195]\n"
        "ds_read_b64_tr_b16 v[178:179], v232 offset:144\n"
        "v_mfma_f32_16x16x32_bf16 v[196:199], v[28:31], v[180:183], v[196:199]\n"
        "ds_read_b64_tr_b16 v[180:181], v232 offset:40\n"
        "s_waitcnt lgkmcnt(11)\n"
        "v_mfma_f32_16x16x32_bf16 v[204:207], v[28:31], v[184:187], v[204:207]\n"
        "ds_read_b64_tr_b16 v[182:183], v232 offset:184\n"
        "v_mfma_f32_16x16x32_bf16 v[200:203], v[24:27], v[184:187], v[200:203]\n"
        "ds_read_b64_tr_b16 v[184:185], v232 offset:72\n"
        "v_mfma_f32_16x16x32_bf16 v[208:211], v[24:27], v[188:191], v[208:211]\n"
        "ds_read_b64_tr_b16 v[186:187], v232 offset:216\n"
        "v_mfma_f32_16x16x32_bf16 v[212:215], v[28:31], v[188:191], v[212:215]\n"
        "ds_read_b64_tr_b16 v[188:189], v232 offset:112\n"
        "s_waitcnt lgkmcnt(11)\n"
        "v_mfma_f32_16x16x32_bf16 v[220:223], v[28:31], v[168:171], v[220:223]\n"
        "ds_read_b64_tr_b16 v[190:191], v232 offset:256\n"
        "v_mfma_f32_16x16x32_bf16 v[216:219], v[24:27], v[168:171], v[216:219]\n"
        "ds_read_b64_tr_b16 v[168:169], v233 offset:0\n"
        "v_mfma_f32_16x16x32_bf16 v[224:227], v[24:27], v[172:175], v[224:227]\n"
        "ds_read_b64_tr_b16 v[170:171], v233 offset:144\n"
        "v_mfma_f32_16x16x32_bf16 v[228:231], v[28:31], v[172:175], v[228:231]\n"
        "ds_read_b64_tr_b16 v[172:173], v232 offset:4608\n"
        "s_waitcnt lgkmcnt(7)\n"
        "v_mfma_f32_16x16x32_bf16 v[128:131], v[176:179], v[96:99], v[128:131]\n"
        "ds_read_b64_tr_b16 v[174:175], v232 offset:4752\n"
        "v_mfma_f32_16x16x32_bf16 v[132:135], v[176:179], v[100:103], v[132:135]\n"
        "ds_read_b64_tr_b16 v[176:177], v232 offset:4648\n"
        "v_mfma_f32_16x16x32_bf16 v[140:143], v[180:183], v[100:103], v[140:143]\n"
        "ds_read_b64_tr_b16 v[178:179], v232 offset:4792\n"
        "v_mfma_f32_16x16x32_bf16 v[136:139], v[180:183], v[96:99], v[136:139]\n"
        "ds_read_b64_tr_b16 v[180:181], v232 offset:4680\n"
        "s_waitcnt lgkmcnt(7)\n"
        "v_mfma_f32_16x16x32_bf16 v[144:147], v[184:187], v[96:99], v[144:147]\n"
        "ds_read_b64_tr_b16 v[182:183], v232 offset:4824\n"
        "v_mfma_f32_16x16x32_bf16 v[148:151], v[184:187], v[100:103], v[148:151]\n"
        "ds_read_b64_tr_b16 v[184:185], v232 offset:4720\n"
        "v_mfma_f32_16x16x32_bf16 v[156:159], v[188:191], v[100:103], v[156:159]\n"
        "ds_read_b64_tr_b16 v[186:187], v232 offset:4864\n"
        "v_mfma_f32_16x16x32_bf16 v[152:155], v[188:191], v[96:99], v[152:155]\n"
        "ds_read_b64_tr_b16 v[188:189], v233 offset:4608\n"
        "s_waitcnt lgkmcnt(7)\n"
        "v_mfma_f32_16x16x32_bf16 v[160:163], v[168:171], v[96:99], v[160:163]\n"
        "ds_read_b64_tr_b16 v[190:191], v233 offset:4752\n"
        "v_mfma_f32_16x16x32_bf16 v[164:167], v[168:171], v[100:103], v[164:167]\n"
        "s_load_dwordx2 s[8:9], s[16:17], 128\n"
        "s_barrier\n"
        "s_waitcnt lgkmcnt(0)\n"
        "ds_read_b64_tr_b16 v[168:169], v234 offset:0\n"
        "v_mfma_f32_16x16x32_bf16 v[128:131], v[172:175], v[104:107], v[128:131]\n"
        "ds_read_b64_tr_b16 v[170:171], v234 offset:144\n"
        "v_mfma_f32_16x16x32_bf16 v[132:135], v[172:175], v[108:111], v[132:135]\n"
        "ds_read_b64_tr_b16 v[172:173], v234 offset:40\n"
        "v_mfma_f32_16x16x32_bf16 v[140:143], v[176:179], v[108:111], v[140:143]\n"
        "ds_read_b64_tr_b16 v[174:175], v234 offset:184\n"
        "v_mfma_f32_16x16x32_bf16 v[136:139], v[176:179], v[104:107], v[136:139]\n"
        "ds_read_b64_tr_b16 v[176:177], v234 offset:72\n"
        "v_mfma_f32_16x16x32_bf16 v[144:147], v[180:183], v[104:107], v[144:147]\n"
        "ds_read_b64_tr_b16 v[178:179], v234 offset:216\n"
        "v_mfma_f32_16x16x32_bf16 v[148:151], v[180:183], v[108:111], v[148:151]\n"
        "ds_read_b64_tr_b16 v[180:181], v234 offset:112\n"
        "v_mfma_f32_16x16x32_bf16 v[156:159], v[184:187], v[108:111], v[156:159]\n"
        "ds_read_b64_tr_b16 v[182:183], v234 offset:256\n"
        "v_mfma_f32_16x16x32_bf16 v[152:155], v[184:187], v[104:107], v[152:155]\n"
        "ds_read_b64_tr_b16 v[184:185], v235 offset:0\n"
        "v_mfma_f32_16x16x32_bf16 v[160:163], v[188:191], v[104:107], v[160:163]\n"
        "ds_read_b64_tr_b16 v[186:187], v235 offset:144\n"
        "v_mfma_f32_16x16x32_bf16 v[164:167], v[188:191], v[108:111], v[164:167]\n"
        "ds_read_b64_tr_b16 v[188:189], v234 offset:4608\n"
        "s_waitcnt lgkmcnt(7)\n"
        "v_mfma_f32_16x16x32_bf16 v[192:195], v[32:35], v[168:171], v[192:195]\n"
        "ds_read_b64_tr_b16 v[190:191], v234 offset:4752\n"
        "v_mfma_f32_16x16x32_bf16 v[196:199], v[36:39], v[168:171], v[196:199]\n"
        "ds_read_b64_tr_b16 v[168:169], v234 offset:4648\n"
        "v_mfma_f32_16x16x32_bf16 v[204:207], v[36:39], v[172:175], v[204:207]\n"
        "ds_read_b64_tr_b16 v[170:171], v234 offset:4792\n"
        "v_mfma_f32_16x16x32_bf16 v[200:203], v[32:35], v[172:175], v[200:203]\n"
        "ds_read_b64_tr_b16 v[172:173], v234 offset:4680\n"
        "s_waitcnt lgkmcnt(7)\n"
        "v_mfma_f32_16x16x32_bf16 v[208:211], v[32:35], v[176:179], v[208:211]\n"
        "ds_read_b64_tr_b16 v[174:175], v234 offset:4824\n"
        "v_mfma_f32_16x16x32_bf16 v[212:215], v[36:39], v[176:179], v[212:215]\n"
        "ds_read_b64_tr_b16 v[176:177], v234 offset:4720\n"
        "v_mfma_f32_16x16x32_bf16 v[220:223], v[36:39], v[180:183], v[220:223]\n"
        "ds_read_b64_tr_b16 v[178:179], v234 offset:4864\n"
        "v_mfma_f32_16x16x32_bf16 v[216:219], v[32:35], v[180:183], v[216:219]\n"
        "Lpw_113:\n"
        "s_xor_b64 s[10:11], s[8:9], s[22:23]\n"
        "s_or_b32 s10, s10, 2\n"
        "s_cmp_eq_u64 s[10:11], 3\n"
        "s_cbranch_scc0 Lpws_114\n"
        "s_and_b32 s57, s8, 2\n"
        "s_lshl_b32 s57, s57, 12\n"
        "s_add_u32 s24, s24, s57\n"
        "s_addc_u32 s25, s25, 0\n"
        "s_add_u32 s28, s28, s57\n"
        "s_addc_u32 s29, s29, 0\n"
        "s_add_u32 s40, s40, s57\n"
        "s_addc_u32 s41, s41, 0\n"
        "ds_read_b64_tr_b16 v[180:181], v235 offset:4608\n"
        "s_waitcnt lgkmcnt(7)\n"
        "v_mfma_f32_16x16x32_bf16 v[224:227], v[32:35], v[184:187], v[224:227]\n"
        "ds_read_b64_tr_b16 v[182:183], v235 offset:4752\n"
        "v_mfma_f32_16x16x32_bf16 v[228:231], v[36:39], v[184:187], v[228:231]\n"
        "s_barrier\n"
        "ds_read_b128 v[112:115], v236 offset:24576\n"
        "ds_read_b128 v[116:119], v236 offset:26624\n"
        "ds_read_b128 v[120:123], v237 offset:24576\n"
        "ds_read_b128 v[124:127], v237 offset:26624\n"
        "ds_read_b64_tr_b16 v[184:185], v232 offset:27648\n"
        "v_mfma_f32_16x16x32_bf16 v[192:195], v[40:43], v[188:191], v[192:195]\n"
        "ds_read_b64_tr_b16 v[186:187], v232 offset:27792\n"
        "v_mfma_f32_16x16x32_bf16 v[196:199], v[44:47], v[188:191], v[196:199]\n"
        "ds_read_b64_tr_b16 v[188:189], v232 offset:27688\n"
        "s_waitcnt lgkmcnt(11)\n"
        "v_mfma_f32_16x16x32_bf16 v[204:207], v[44:47], v[168:171], v[204:207]\n"
        "ds_read_b64_tr_b16 v[190:191], v232 offset:27832\n"
        "v_mfma_f32_16x16x32_bf16 v[200:203], v[40:43], v[168:171], v[200:203]\n"
        "ds_read_b64_tr_b16 v[168:169], v232 offset:27720\n"
        "v_mfma_f32_16x16x32_bf16 v[208:211], v[40:43], v[172:175], v[208:211]\n"
        "ds_read_b64_tr_b16 v[170:171], v232 offset:27864\n"
        "v_mfma_f32_16x16x32_bf16 v[212:215], v[44:47], v[172:175], v[212:215]\n"
        "ds_read_b64_tr_b16 v[172:173], v232 offset:27760\n"
        "s_waitcnt lgkmcnt(11)\n"
        "v_mfma_f32_16x16x32_bf16 v[220:223], v[44:47], v[176:179], v[220:223]\n"
        "ds_read_b64_tr_b16 v[174:175], v232 offset:27904\n"
        "v_mfma_f32_16x16x32_bf16 v[216:219], v[40:43], v[176:179], v[216:219]\n"
        "ds_read_b64_tr_b16 v[176:177], v233 offset:27648\n"
        "v_mfma_f32_16x16x32_bf16 v[224:227], v[40:43], v[180:183], v[224:227]\n"
        "ds_read_b64_tr_b16 v[178:179], v233 offset:27792\n"
        "v_mfma_f32_16x16x32_bf16 v[228:231], v[44:47], v[180:183], v[228:231]\n"
        "ds_read_b64_tr_b16 v[180:181], v232 offset:32256\n"
        "s_waitcnt lgkmcnt(7)\n"
        "v_mfma_f32_16x16x32_bf16 v[128:131], v[184:187], v[112:115], v[128:131]\n"
        "ds_read_b64_tr_b16 v[182:183], v232 offset:32400\n"
        "v_mfma_f32_16x16x32_bf16 v[132:135], v[184:187], v[116:119], v[132:135]\n"
        "ds_read_b64_tr_b16 v[184:185], v232 offset:32296\n"
        "v_mfma_f32_16x16x32_bf16 v[140:143], v[188:191], v[116:119], v[140:143]\n"
        "ds_read_b64_tr_b16 v[186:187], v232 offset:32440\n"
        "v_mfma_f32_16x16x32_bf16 v[136:139], v[188:191], v[112:115], v[136:139]\n"
        "ds_read_b64_tr_b16 v[188:189], v232 offset:32328\n"
        "s_waitcnt lgkmcnt(7)\n"
        "v_mfma_f32_16x16x32_bf16 v[144:147], v[168:171], v[112:115], v[144:147]\n"
        "ds_read_b64_tr_b16 v[190:191], v232 offset:32472\n"
        "v_mfma_f32_16x16x32_bf16 v[148:151], v[168:171], v[116:119], v[148:151]\n"
        "ds_read_b64_tr_b16 v[168:169], v232 offset:32368\n"
        "v_mfma_f32_16x16x32_bf16 v[156:159], v[172:175], v[116:119], v[156:159]\n"
        "ds_read_b64_tr_b16 v[170:171], v232 offset:32512\n"
        "v_mfma_f32_16x16x32_bf16 v[152:155], v[172:175], v[112:115], v[152:155]\n"
        "ds_read_b64_tr_b16 v[172:173], v233 offset:32256\n"
        "s_waitcnt lgkmcnt(7)\n"
        "v_mfma_f32_16x16x32_bf16 v[160:163], v[176:179], v[112:115], v[160:163]\n"
        "ds_read_b64_tr_b16 v[174:175], v233 offset:32400\n"
        "v_mfma_f32_16x16x32_bf16 v[164:167], v[176:179], v[116:119], v[164:167]\n"
        "s_barrier\n"
        "ds_read_b64_tr_b16 v[176:177], v232 offset:18432\n"
        "v_mfma_f32_16x16x32_bf16 v[128:131], v[180:183], v[120:123], v[128:131]\n"
        "ds_read_b64_tr_b16 v[178:179], v232 offset:18576\n"
        "v_mfma_f32_16x16x32_bf16 v[132:135], v[180:183], v[124:127], v[132:135]\n"
        "ds_read_b64_tr_b16 v[180:181], v232 offset:18472\n"
        "s_waitcnt lgkmcnt(7)\n"
        "v_mfma_f32_16x16x32_bf16 v[140:143], v[184:187], v[124:127], v[140:143]\n"
        "ds_read_b64_tr_b16 v[182:183], v232 offset:18616\n"
        "v_mfma_f32_16x16x32_bf16 v[136:139], v[184:187], v[120:123], v[136:139]\n"
        "ds_read_b64_tr_b16 v[184:185], v232 offset:18504\n"
        "v_mfma_f32_16x16x32_bf16 v[144:147], v[188:191], v[120:123], v[144:147]\n"
        "ds_read_b64_tr_b16 v[186:187], v232 offset:18648\n"
        "v_mfma_f32_16x16x32_bf16 v[148:151], v[188:191], v[124:127], v[148:151]\n"
        "ds_read_b64_tr_b16 v[188:189], v232 offset:18544\n"
        "s_waitcnt lgkmcnt(7)\n"
        "v_mfma_f32_16x16x32_bf16 v[156:159], v[168:171], v[124:127], v[156:159]\n"
        "ds_read_b64_tr_b16 v[190:191], v232 offset:18688\n"
        "v_mfma_f32_16x16x32_bf16 v[152:155], v[168:171], v[120:123], v[152:155]\n"
        "ds_read_b64_tr_b16 v[168:169], v233 offset:18432\n"
        "v_mfma_f32_16x16x32_bf16 v[160:163], v[172:175], v[120:123], v[160:163]\n"
        "ds_read_b64_tr_b16 v[170:171], v233 offset:18576\n"
        "v_mfma_f32_16x16x32_bf16 v[164:167], v[172:175], v[124:127], v[164:167]\n"
        "ds_read_b64_tr_b16 v[172:173], v232 offset:23040\n"
        "s_waitcnt lgkmcnt(7)\n"
        "v_mfma_f32_16x16x32_bf16 v[192:195], v[48:51], v[176:179], v[192:195]\n"
        "ds_read_b64_tr_b16 v[174:175], v232 offset:23184\n"
        "v_mfma_f32_16x16x32_bf16 v[196:199], v[52:55], v[176:179], v[196:199]\n"
        "ds_read_b64_tr_b16 v[176:177], v232 offset:23080\n"
        "v_mfma_f32_16x16x32_bf16 v[204:207], v[52:55], v[180:183], v[204:207]\n"
        "ds_read_b64_tr_b16 v[178:179], v232 offset:23224\n"
        "v_mfma_f32_16x16x32_bf16 v[200:203], v[48:51], v[180:183], v[200:203]\n"
        "ds_read_b64_tr_b16 v[180:181], v232 offset:23112\n"
        "s_waitcnt lgkmcnt(7)\n"
        "v_mfma_f32_16x16x32_bf16 v[208:211], v[48:51], v[184:187], v[208:211]\n"
        "ds_read_b64_tr_b16 v[182:183], v232 offset:23256\n"
        "v_mfma_f32_16x16x32_bf16 v[212:215], v[52:55], v[184:187], v[212:215]\n"
        "ds_read_b64_tr_b16 v[184:185], v232 offset:23152\n"
        "v_mfma_f32_16x16x32_bf16 v[220:223], v[52:55], v[188:191], v[220:223]\n"
        "ds_read_b64_tr_b16 v[186:187], v232 offset:23296\n"
        "v_mfma_f32_16x16x32_bf16 v[216:219], v[48:51], v[188:191], v[216:219]\n"
        "ds_read_b64_tr_b16 v[188:189], v233 offset:23040\n"
        "s_waitcnt lgkmcnt(7)\n"
        "v_mfma_f32_16x16x32_bf16 v[224:227], v[48:51], v[168:171], v[224:227]\n"
        "ds_read_b64_tr_b16 v[190:191], v233 offset:23184\n"
        "v_mfma_f32_16x16x32_bf16 v[228:231], v[52:55], v[168:171], v[228:231]\n"
        "s_barrier\n"
        "ds_read_b128 v[32:35], v236 offset:49152\n"
        "ds_read_b128 v[36:39], v236 offset:51200\n"
        "ds_read_b128 v[40:43], v237 offset:49152\n"
        "ds_read_b128 v[44:47], v237 offset:51200\n"
        "ds_read_b64_tr_b16 v[168:169], v232 offset:9216\n"
        "v_mfma_f32_16x16x32_bf16 v[192:195], v[56:59], v[172:175], v[192:195]\n"
        "ds_read_b64_tr_b16 v[170:171], v232 offset:9360\n"
        "v_mfma_f32_16x16x32_bf16 v[196:199], v[60:63], v[172:175], v[196:199]\n"
        "ds_read_b64_tr_b16 v[172:173], v232 offset:9256\n"
        "s_waitcnt lgkmcnt(11)\n"
        "v_mfma_f32_16x16x32_bf16 v[204:207], v[60:63], v[176:179], v[204:207]\n"
        "ds_read_b64_tr_b16 v[174:175], v232 offset:9400\n"
        "v_mfma_f32_16x16x32_bf16 v[200:203], v[56:59], v[176:179], v[200:203]\n"
        "ds_read_b64_tr_b16 v[176:177], v232 offset:9288\n"
        "v_mfma_f32_16x16x32_bf16 v[208:211], v[56:59], v[180:183], v[208:211]\n"
        "ds_read_b64_tr_b16 v[178:179], v232 offset:9432\n"
        "v_mfma_f32_16x16x32_bf16 v[212:215], v[60:63], v[180:183], v[212:215]\n"
        "ds_read_b64_tr_b16 v[180:181], v232 offset:9328\n"
        "s_waitcnt lgkmcnt(11)\n"
        "v_mfma_f32_16x16x32_bf16 v[220:223], v[60:63], v[184:187], v[220:223]\n"
        "ds_read_b64_tr_b16 v[182:183], v232 offset:9472\n"
        "v_mfma_f32_16x16x32_bf16 v[216:219], v[56:59], v[184:187], v[216:219]\n"
        "ds_read_b64_tr_b16 v[184:185], v233 offset:9216\n"
        "v_mfma_f32_16x16x32_bf16 v[224:227], v[56:59], v[188:191], v[224:227]\n"
        "ds_read_b64_tr_b16 v[186:187], v233 offset:9360\n"
        "v_mfma_f32_16x16x32_bf16 v[228:231], v[60:63], v[188:191], v[228:231]\n"
        "ds_read_b64_tr_b16 v[188:189], v232 offset:13824\n"
        "s_waitcnt lgkmcnt(7)\n"
        "v_mfma_f32_16x16x32_bf16 v[128:131], v[168:171], v[32:35], v[128:131]\n"
        "ds_read_b64_tr_b16 v[190:191], v232 offset:13968\n"
        "v_mfma_f32_16x16x32_bf16 v[132:135], v[168:171], v[36:39], v[132:135]\n"
        "ds_read_b64_tr_b16 v[168:169], v232 offset:13864\n"
        "v_mfma_f32_16x16x32_bf16 v[140:143], v[172:175], v[36:39], v[140:143]\n"
        "ds_read_b64_tr_b16 v[170:171], v232 offset:14008\n"
        "v_mfma_f32_16x16x32_bf16 v[136:139], v[172:175], v[32:35], v[136:139]\n"
        "ds_read_b64_tr_b16 v[172:173], v232 offset:13896\n"
        "s_waitcnt lgkmcnt(7)\n"
        "v_mfma_f32_16x16x32_bf16 v[144:147], v[176:179], v[32:35], v[144:147]\n"
        "ds_read_b64_tr_b16 v[174:175], v232 offset:14040\n"
        "v_mfma_f32_16x16x32_bf16 v[148:151], v[176:179], v[36:39], v[148:151]\n"
        "ds_read_b64_tr_b16 v[176:177], v232 offset:13936\n"
        "v_mfma_f32_16x16x32_bf16 v[156:159], v[180:183], v[36:39], v[156:159]\n"
        "ds_read_b64_tr_b16 v[178:179], v232 offset:14080\n"
        "v_mfma_f32_16x16x32_bf16 v[152:155], v[180:183], v[32:35], v[152:155]\n"
        "ds_read_b64_tr_b16 v[180:181], v233 offset:13824\n"
        "s_waitcnt lgkmcnt(7)\n"
        "v_mfma_f32_16x16x32_bf16 v[160:163], v[184:187], v[32:35], v[160:163]\n"
        "ds_read_b64_tr_b16 v[182:183], v233 offset:13968\n"
        "v_mfma_f32_16x16x32_bf16 v[164:167], v[184:187], v[36:39], v[164:167]\n"
        "s_barrier\n"
        "ds_read_b64_tr_b16 v[184:185], v232 offset:0\n"
        "v_mfma_f32_16x16x32_bf16 v[128:131], v[188:191], v[40:43], v[128:131]\n"
        "ds_read_b64_tr_b16 v[186:187], v232 offset:144\n"
        "v_mfma_f32_16x16x32_bf16 v[132:135], v[188:191], v[44:47], v[132:135]\n"
        "ds_read_b64_tr_b16 v[188:189], v232 offset:40\n"
        "s_waitcnt lgkmcnt(7)\n"
        "v_mfma_f32_16x16x32_bf16 v[140:143], v[168:171], v[44:47], v[140:143]\n"
        "ds_read_b64_tr_b16 v[190:191], v232 offset:184\n"
        "v_mfma_f32_16x16x32_bf16 v[136:139], v[168:171], v[40:43], v[136:139]\n"
        "ds_read_b64_tr_b16 v[168:169], v232 offset:72\n"
        "v_mfma_f32_16x16x32_bf16 v[144:147], v[172:175], v[40:43], v[144:147]\n"
        "ds_read_b64_tr_b16 v[170:171], v232 offset:216\n"
        "v_mfma_f32_16x16x32_bf16 v[148:151], v[172:175], v[44:47], v[148:151]\n"
        "ds_read_b64_tr_b16 v[172:173], v232 offset:112\n"
        "s_waitcnt lgkmcnt(7)\n"
        "v_mfma_f32_16x16x32_bf16 v[156:159], v[176:179], v[44:47], v[156:159]\n"
        "ds_read_b64_tr_b16 v[174:175], v232 offset:256\n"
        "v_mfma_f32_16x16x32_bf16 v[152:155], v[176:179], v[40:43], v[152:155]\n"
        "ds_read_b64_tr_b16 v[176:177], v233 offset:0\n"
        "v_mfma_f32_16x16x32_bf16 v[160:163], v[180:183], v[40:43], v[160:163]\n"
        "ds_read_b64_tr_b16 v[178:179], v233 offset:144\n"
        "v_mfma_f32_16x16x32_bf16 v[164:167], v[180:183], v[44:47], v[164:167]\n"
        "v_pk_mul_f32 v[128:129], v[128:129], s[60:61]\n"
        "v_pk_mul_f32 v[130:131], v[130:131], s[60:61]\n"
        "v_pk_mul_f32 v[132:133], v[132:133], s[60:61]\n"
        "v_pk_mul_f32 v[134:135], v[134:135], s[60:61]\n"
        "v_cvt_pk_bf16_f32 v128, v128, v129\n"
        "v_cvt_pk_bf16_f32 v129, v130, v131\n"
        "v_cvt_pk_bf16_f32 v130, v132, v133\n"
        "v_cvt_pk_bf16_f32 v131, v134, v135\n"
        "v_pk_mul_f32 v[136:137], v[136:137], s[60:61]\n"
        "v_pk_mul_f32 v[138:139], v[138:139], s[60:61]\n"
        "v_pk_mul_f32 v[140:141], v[140:141], s[60:61]\n"
        "v_pk_mul_f32 v[142:143], v[142:143], s[60:61]\n"
        "v_cvt_pk_bf16_f32 v136, v136, v137\n"
        "v_cvt_pk_bf16_f32 v137, v138, v139\n"
        "v_cvt_pk_bf16_f32 v138, v140, v141\n"
        "v_cvt_pk_bf16_f32 v139, v142, v143\n"
        "v_permlane16_swap_b32 v128, v130\n"
        "v_permlane16_swap_b32 v129, v131\n"
        "ds_write_b128 v238, v[128:131] offset:16384\n"
        "v_pk_mul_f32 v[144:145], v[144:145], s[60:61]\n"
        "v_pk_mul_f32 v[146:147], v[146:147], s[60:61]\n"
        "v_pk_mul_f32 v[148:149], v[148:149], s[60:61]\n"
        "v_pk_mul_f32 v[150:151], v[150:151], s[60:61]\n"
        "v_cvt_pk_bf16_f32 v144, v144, v145\n"
        "v_cvt_pk_bf16_f32 v145, v146, v147\n"
        "v_cvt_pk_bf16_f32 v146, v148, v149\n"
        "v_cvt_pk_bf16_f32 v147, v150, v151\n"
        "v_permlane16_swap_b32 v136, v138\n"
        "v_permlane16_swap_b32 v137, v139\n"
        "ds_write_b128 v238, v[136:139] offset:40960\n"
        "v_pk_mul_f32 v[152:153], v[152:153], s[60:61]\n"
        "v_pk_mul_f32 v[154:155], v[154:155], s[60:61]\n"
        "v_pk_mul_f32 v[156:157], v[156:157], s[60:61]\n"
        "v_pk_mul_f32 v[158:159], v[158:159], s[60:61]\n"
        "v_cvt_pk_bf16_f32 v152, v152, v153\n"
        "v_cvt_pk_bf16_f32 v153, v154, v155\n"
        "v_cvt_pk_bf16_f32 v154, v156, v157\n"
        "v_cvt_pk_bf16_f32 v155, v158, v159\n"
        "v_permlane16_swap_b32 v144, v146\n"
        "v_permlane16_swap_b32 v145, v147\n"
        "ds_write_b128 v239, v[144:147] offset:16384\n"
        "v_pk_mul_f32 v[160:161], v[160:161], s[60:61]\n"
        "v_pk_mul_f32 v[162:163], v[162:163], s[60:61]\n"
        "v_pk_mul_f32 v[164:165], v[164:165], s[60:61]\n"
        "v_pk_mul_f32 v[166:167], v[166:167], s[60:61]\n"
        "v_cvt_pk_bf16_f32 v160, v160, v161\n"
        "v_cvt_pk_bf16_f32 v161, v162, v163\n"
        "v_cvt_pk_bf16_f32 v162, v164, v165\n"
        "v_cvt_pk_bf16_f32 v163, v166, v167\n"
        "v_permlane16_swap_b32 v152, v154\n"
        "v_permlane16_swap_b32 v153, v155\n"
        "ds_write_b128 v239, v[152:155] offset:40960\n"
        "v_permlane16_swap_b32 v160, v162\n"
        "v_permlane16_swap_b32 v161, v163\n"
        "s_nop 0\n"
        "v_permlane32_swap_b32 v160, v162\n"
        "v_permlane32_swap_b32 v161, v163\n"
        "ds_write_b64 v240, v[160:161] offset:16384\n"
        "ds_read_b64_tr_b16 v[180:181], v232 offset:4608\n"
        "s_waitcnt lgkmcnt(12)\n"
        "v_mfma_f32_16x16x32_bf16 v[192:195], v[64:67], v[184:187], v[192:195]\n"
        "ds_read_b64_tr_b16 v[182:183], v232 offset:4752\n"
        "v_mfma_f32_16x16x32_bf16 v[196:199], v[68:71], v[184:187], v[196:199]\n"
        "ds_read_b64_tr_b16 v[184:185], v232 offset:4648\n"
        "v_mfma_f32_16x16x32_bf16 v[204:207], v[68:71], v[188:191], v[204:207]\n"
        "ds_read_b64_tr_b16 v[186:187], v232 offset:4792\n"
        "v_mfma_f32_16x16x32_bf16 v[200:203], v[64:67], v[188:191], v[200:203]\n"
        "ds_read_b64_tr_b16 v[188:189], v232 offset:4680\n"
        "s_waitcnt lgkmcnt(12)\n"
        "v_mfma_f32_16x16x32_bf16 v[208:211], v[64:67], v[168:171], v[208:211]\n"
        "ds_read_b64_tr_b16 v[190:191], v232 offset:4824\n"
        "v_mfma_f32_16x16x32_bf16 v[212:215], v[68:71], v[168:171], v[212:215]\n"
        "ds_read_b64_tr_b16 v[168:169], v232 offset:4720\n"
        "v_mfma_f32_16x16x32_bf16 v[220:223], v[68:71], v[172:175], v[220:223]\n"
        "ds_read_b64_tr_b16 v[170:171], v232 offset:4864\n"
        "v_mfma_f32_16x16x32_bf16 v[216:219], v[64:67], v[172:175], v[216:219]\n"
        "ds_read_b64_tr_b16 v[172:173], v233 offset:4608\n"
        "s_waitcnt lgkmcnt(7)\n"
        "v_mfma_f32_16x16x32_bf16 v[224:227], v[64:67], v[176:179], v[224:227]\n"
        "ds_read_b64_tr_b16 v[174:175], v233 offset:4752\n"
        "v_mfma_f32_16x16x32_bf16 v[228:231], v[68:71], v[176:179], v[228:231]\n"
        "s_barrier\n"
        "ds_read_b64_tr_b16 v[176:177], v234 offset:49152\n"
        "v_mfma_f32_16x16x32_bf16 v[192:195], v[72:75], v[180:183], v[192:195]\n"
        "ds_read_b64_tr_b16 v[178:179], v234 offset:49296\n"
        "v_mfma_f32_16x16x32_bf16 v[196:199], v[76:79], v[180:183], v[196:199]\n"
        "ds_read_b64_tr_b16 v[180:181], v234 offset:49192\n"
        "s_waitcnt lgkmcnt(7)\n"
        "v_mfma_f32_16x16x32_bf16 v[204:207], v[76:79], v[184:187], v[204:207]\n"
        "ds_read_b64_tr_b16 v[182:183], v234 offset:49336\n"
        "v_mfma_f32_16x16x32_bf16 v[200:203], v[72:75], v[184:187], v[200:203]\n"
        "ds_read_b64_tr_b16 v[184:185], v234 offset:49224\n"
        "v_mfma_f32_16x16x32_bf16 v[208:211], v[72:75], v[188:191], v[208:211]\n"
        "ds_read_b64_tr_b16 v[186:187], v234 offset:49368\n"
        "v_mfma_f32_16x16x32_bf16 v[212:215], v[76:79], v[188:191], v[212:215]\n"
        "ds_read_b64_tr_b16 v[188:189], v234 offset:49264\n"
        "s_waitcnt lgkmcnt(7)\n"
        "v_mfma_f32_16x16x32_bf16 v[220:223], v[76:79], v[168:171], v[220:223]\n"
        "ds_read_b64_tr_b16 v[190:191], v234 offset:49408\n"
        "v_mfma_f32_16x16x32_bf16 v[216:219], v[72:75], v[168:171], v[216:219]\n"
        "ds_read_b64_tr_b16 v[168:169], v235 offset:49152\n"
        "v_mfma_f32_16x16x32_bf16 v[224:227], v[72:75], v[172:175], v[224:227]\n"
        "ds_read_b64_tr_b16 v[170:171], v235 offset:49296\n"
        "v_mfma_f32_16x16x32_bf16 v[228:231], v[76:79], v[172:175], v[228:231]\n"
        "ds_read_b64_tr_b16 v[172:173], v234 offset:53760\n"
        "s_waitcnt lgkmcnt(7)\n"
        "v_mfma_f32_16x16x32_bf16 v[192:195], v[80:83], v[176:179], v[192:195]\n"
        "ds_read_b64_tr_b16 v[174:175], v234 offset:53904\n"
        "v_mfma_f32_16x16x32_bf16 v[196:199], v[84:87], v[176:179], v[196:199]\n"
        "ds_read_b64_tr_b16 v[176:177], v234 offset:53800\n"
        "v_mfma_f32_16x16x32_bf16 v[204:207], v[84:87], v[180:183], v[204:207]\n"
        "ds_read_b64_tr_b16 v[178:179], v234 offset:53944\n"
        "v_mfma_f32_16x16x32_bf16 v[200:203], v[80:83], v[180:183], v[200:203]\n"
        "ds_read_b64_tr_b16 v[180:181], v234 offset:53832\n"
        "s_waitcnt lgkmcnt(7)\n"
        "v_mfma_f32_16x16x32_bf16 v[208:211], v[80:83], v[184:187], v[208:211]\n"
        "ds_read_b64_tr_b16 v[182:183], v234 offset:53976\n"
        "v_mfma_f32_16x16x32_bf16 v[212:215], v[84:87], v[184:187], v[212:215]\n"
        "ds_read_b64_tr_b16 v[184:185], v234 offset:53872\n"
        "v_mfma_f32_16x16x32_bf16 v[220:223], v[84:87], v[188:191], v[220:223]\n"
        "ds_read_b64_tr_b16 v[186:187], v234 offset:54016\n"
        "v_mfma_f32_16x16x32_bf16 v[216:219], v[80:83], v[188:191], v[216:219]\n"
        "ds_read_b64_tr_b16 v[188:189], v235 offset:53760\n"
        "s_waitcnt lgkmcnt(7)\n"
        "v_mfma_f32_16x16x32_bf16 v[224:227], v[80:83], v[168:171], v[224:227]\n"
        "ds_read_b64_tr_b16 v[190:191], v235 offset:53904\n"
        "v_mfma_f32_16x16x32_bf16 v[228:231], v[84:87], v[168:171], v[228:231]\n"
        "s_barrier\n"
        "ds_read_b64_tr_b16 v[168:169], v234 offset:0\n"
        "v_mfma_f32_16x16x32_bf16 v[192:195], v[88:91], v[172:175], v[192:195]\n"
        "ds_read_b64_tr_b16 v[170:171], v234 offset:144\n"
        "v_mfma_f32_16x16x32_bf16 v[196:199], v[92:95], v[172:175], v[196:199]\n"
        "ds_read_b64_tr_b16 v[172:173], v234 offset:40\n"
        "s_waitcnt lgkmcnt(7)\n"
        "v_mfma_f32_16x16x32_bf16 v[204:207], v[92:95], v[176:179], v[204:207]\n"
        "ds_read_b64_tr_b16 v[174:175], v234 offset:184\n"
        "v_mfma_f32_16x16x32_bf16 v[200:203], v[88:91], v[176:179], v[200:203]\n"
        "ds_read_b64_tr_b16 v[176:177], v234 offset:72\n"
        "v_mfma_f32_16x16x32_bf16 v[208:211], v[88:91], v[180:183], v[208:211]\n"
        "ds_read_b64_tr_b16 v[178:179], v234 offset:216\n"
        "v_mfma_f32_16x16x32_bf16 v[212:215], v[92:95], v[180:183], v[212:215]\n"
        "ds_read_b64_tr_b16 v[180:181], v234 offset:112\n"
        "s_waitcnt lgkmcnt(7)\n"
        "v_mfma_f32_16x16x32_bf16 v[220:223], v[92:95], v[184:187], v[220:223]\n"
        "ds_read_b64_tr_b16 v[182:183], v234 offset:256\n"
        "v_mfma_f32_16x16x32_bf16 v[216:219], v[88:91], v[184:187], v[216:219]\n"
        "ds_read_b64_tr_b16 v[184:185], v235 offset:0\n"
        "v_mfma_f32_16x16x32_bf16 v[224:227], v[88:91], v[188:191], v[224:227]\n"
        "ds_read_b64_tr_b16 v[186:187], v235 offset:144\n"
        "v_mfma_f32_16x16x32_bf16 v[228:231], v[92:95], v[188:191], v[228:231]\n"
        "ds_read_b64_tr_b16 v[188:189], v234 offset:4608\n"
        "s_waitcnt lgkmcnt(7)\n"
        "v_mfma_f32_16x16x32_bf16 v[192:195], v[96:99], v[168:171], v[192:195]\n"
        "ds_read_b64_tr_b16 v[190:191], v234 offset:4752\n"
        "v_mfma_f32_16x16x32_bf16 v[196:199], v[100:103], v[168:171], v[196:199]\n"
        "ds_read_b64_tr_b16 v[168:169], v234 offset:4648\n"
        "v_mfma_f32_16x16x32_bf16 v[204:207], v[100:103], v[172:175], v[204:207]\n"
        "ds_read_b64_tr_b16 v[170:171], v234 offset:4792\n"
        "v_mfma_f32_16x16x32_bf16 v[200:203], v[96:99], v[172:175], v[200:203]\n"
        "ds_read_b64_tr_b16 v[172:173], v234 offset:4680\n"
        "s_waitcnt lgkmcnt(7)\n"
        "v_mfma_f32_16x16x32_bf16 v[208:211], v[96:99], v[176:179], v[208:211]\n"
        "ds_read_b64_tr_b16 v[174:175], v234 offset:4824\n"
        "v_mfma_f32_16x16x32_bf16 v[212:215], v[100:103], v[176:179], v[212:215]\n"
        "ds_read_b64_tr_b16 v[176:177], v234 offset:4720\n"
        "v_mfma_f32_16x16x32_bf16 v[220:223], v[100:103], v[180:183], v[220:223]\n"
        "ds_read_b64_tr_b16 v[178:179], v234 offset:4864\n"
        "v_mfma_f32_16x16x32_bf16 v[216:219], v[96:99], v[180:183], v[216:219]\n"
        "ds_read_b64_tr_b16 v[180:181], v235 offset:4608\n"
        "s_waitcnt lgkmcnt(7)\n"
        "v_mfma_f32_16x16x32_bf16 v[224:227], v[96:99], v[184:187], v[224:227]\n"
        "ds_read_b64_tr_b16 v[182:183], v235 offset:4752\n"
        "v_mfma_f32_16x16x32_bf16 v[228:231], v[100:103], v[184:187], v[228:231]\n"
        "s_barrier\n"
        "ds_read_b64_tr_b16 v[184:185], v232 offset:27648\n"
        "v_mfma_f32_16x16x32_bf16 v[192:195], v[104:107], v[188:191], v[192:195]\n"
        "ds_read_b64_tr_b16 v[186:187], v232 offset:27792\n"
        "v_mfma_f32_16x16x32_bf16 v[196:199], v[108:111], v[188:191], v[196:199]\n"
        "ds_read_b64_tr_b16 v[188:189], v232 offset:27688\n"
        "s_waitcnt lgkmcnt(7)\n"
        "v_mfma_f32_16x16x32_bf16 v[204:207], v[108:111], v[168:171], v[204:207]\n"
        "ds_read_b64_tr_b16 v[190:191], v232 offset:27832\n"
        "v_mfma_f32_16x16x32_bf16 v[200:203], v[104:107], v[168:171], v[200:203]\n"
        "ds_read_b64_tr_b16 v[168:169], v232 offset:27720\n"
        "v_mfma_f32_16x16x32_bf16 v[208:211], v[104:107], v[172:175], v[208:211]\n"
        "ds_read_b64_tr_b16 v[170:171], v232 offset:27864\n"
        "v_mfma_f32_16x16x32_bf16 v[212:215], v[108:111], v[172:175], v[212:215]\n"
        "ds_read_b64_tr_b16 v[172:173], v232 offset:27760\n"
        "s_waitcnt lgkmcnt(7)\n"
        "v_mfma_f32_16x16x32_bf16 v[220:223], v[108:111], v[176:179], v[220:223]\n"
        "ds_read_b64_tr_b16 v[174:175], v232 offset:27904\n"
        "v_mfma_f32_16x16x32_bf16 v[216:219], v[104:107], v[176:179], v[216:219]\n"
        "ds_read_b64_tr_b16 v[176:177], v233 offset:27648\n"
        "v_mfma_f32_16x16x32_bf16 v[224:227], v[104:107], v[180:183], v[224:227]\n"
        "ds_read_b64_tr_b16 v[178:179], v233 offset:27792\n"
        "v_mfma_f32_16x16x32_bf16 v[228:231], v[108:111], v[180:183], v[228:231]\n"
        "ds_read_b64_tr_b16 v[180:181], v232 offset:32256\n"
        "s_waitcnt lgkmcnt(7)\n"
        "v_mfma_f32_16x16x32_bf16 v[192:195], v[112:115], v[184:187], v[192:195]\n"
        "ds_read_b64_tr_b16 v[182:183], v232 offset:32400\n"
        "v_mfma_f32_16x16x32_bf16 v[196:199], v[116:119], v[184:187], v[196:199]\n"
        "ds_read_b64_tr_b16 v[184:185], v232 offset:32296\n"
        "v_mfma_f32_16x16x32_bf16 v[204:207], v[116:119], v[188:191], v[204:207]\n"
        "ds_read_b64_tr_b16 v[186:187], v232 offset:32440\n"
        "v_mfma_f32_16x16x32_bf16 v[200:203], v[112:115], v[188:191], v[200:203]\n"
        "ds_read_b64_tr_b16 v[188:189], v232 offset:32328\n"
        "s_waitcnt lgkmcnt(7)\n"
        "v_mfma_f32_16x16x32_bf16 v[208:211], v[112:115], v[168:171], v[208:211]\n"
        "ds_read_b64_tr_b16 v[190:191], v232 offset:32472\n"
        "v_mfma_f32_16x16x32_bf16 v[212:215], v[116:119], v[168:171], v[212:215]\n"
        "ds_read_b64_tr_b16 v[168:169], v232 offset:32368\n"
        "v_mfma_f32_16x16x32_bf16 v[220:223], v[116:119], v[172:175], v[220:223]\n"
        "ds_read_b64_tr_b16 v[170:171], v232 offset:32512\n"
        "v_mfma_f32_16x16x32_bf16 v[216:219], v[112:115], v[172:175], v[216:219]\n"
        "ds_read_b64_tr_b16 v[172:173], v233 offset:32256\n"
        "s_waitcnt lgkmcnt(7)\n"
        "v_mfma_f32_16x16x32_bf16 v[224:227], v[112:115], v[176:179], v[224:227]\n"
        "ds_read_b64_tr_b16 v[174:175], v233 offset:32400\n"
        "v_mfma_f32_16x16x32_bf16 v[228:231], v[116:119], v[176:179], v[228:231]\n"
        "s_barrier\n"
        "ds_read_b64_tr_b16 v[176:177], v232 offset:18432\n"
        "v_mfma_f32_16x16x32_bf16 v[192:195], v[120:123], v[180:183], v[192:195]\n"
        "ds_read_b64_tr_b16 v[178:179], v232 offset:18576\n"
        "v_mfma_f32_16x16x32_bf16 v[196:199], v[124:127], v[180:183], v[196:199]\n"
        "ds_read_b64_tr_b16 v[180:181], v232 offset:18472\n"
        "s_waitcnt lgkmcnt(7)\n"
        "v_mfma_f32_16x16x32_bf16 v[204:207], v[124:127], v[184:187], v[204:207]\n"
        "ds_read_b64_tr_b16 v[182:183], v232 offset:18616\n"
        "v_mfma_f32_16x16x32_bf16 v[200:203], v[120:123], v[184:187], v[200:203]\n"
        "ds_read_b64_tr_b16 v[184:185], v232 offset:18504\n"
        "v_mfma_f32_16x16x32_bf16 v[208:211], v[120:123], v[188:191], v[208:211]\n"
        "ds_read_b64_tr_b16 v[186:187], v232 offset:18648\n"
        "v_mfma_f32_16x16x32_bf16 v[212:215], v[124:127], v[188:191], v[212:215]\n"
        "ds_read_b64_tr_b16 v[188:189], v232 offset:18544\n"
        "s_waitcnt lgkmcnt(7)\n"
        "v_mfma_f32_16x16x32_bf16 v[220:223], v[124:127], v[168:171], v[220:223]\n"
        "ds_read_b64_tr_b16 v[190:191], v232 offset:18688\n"
        "v_mfma_f32_16x16x32_bf16 v[216:219], v[120:123], v[168:171], v[216:219]\n"
        "ds_read_b64_tr_b16 v[168:169], v233 offset:18432\n"
        "v_mfma_f32_16x16x32_bf16 v[224:227], v[120:123], v[172:175], v[224:227]\n"
        "ds_read_b64_tr_b16 v[170:171], v233 offset:18576\n"
        "v_mfma_f32_16x16x32_bf16 v[228:231], v[124:127], v[172:175], v[228:231]\n"
        "ds_read_b64_tr_b16 v[172:173], v232 offset:23040\n"
        "s_waitcnt lgkmcnt(7)\n"
        "v_mfma_f32_16x16x32_bf16 v[192:195], v[32:35], v[176:179], v[192:195]\n"
        "ds_read_b64_tr_b16 v[174:175], v232 offset:23184\n"
        "v_mfma_f32_16x16x32_bf16 v[196:199], v[36:39], v[176:179], v[196:199]\n"
        "ds_read_b64_tr_b16 v[176:177], v232 offset:23080\n"
        "v_mfma_f32_16x16x32_bf16 v[204:207], v[36:39], v[180:183], v[204:207]\n"
        "ds_read_b64_tr_b16 v[178:179], v232 offset:23224\n"
        "v_mfma_f32_16x16x32_bf16 v[200:203], v[32:35], v[180:183], v[200:203]\n"
        "ds_read_b64_tr_b16 v[180:181], v232 offset:23112\n"
        "s_waitcnt lgkmcnt(7)\n"
        "v_mfma_f32_16x16x32_bf16 v[208:211], v[32:35], v[184:187], v[208:211]\n"
        "ds_read_b64_tr_b16 v[182:183], v232 offset:23256\n"
        "v_mfma_f32_16x16x32_bf16 v[212:215], v[36:39], v[184:187], v[212:215]\n"
        "ds_read_b64_tr_b16 v[184:185], v232 offset:23152\n"
        "v_mfma_f32_16x16x32_bf16 v[220:223], v[36:39], v[188:191], v[220:223]\n"
        "ds_read_b64_tr_b16 v[186:187], v232 offset:23296\n"
        "v_mfma_f32_16x16x32_bf16 v[216:219], v[32:35], v[188:191], v[216:219]\n"
        "ds_read_b64_tr_b16 v[188:189], v233 offset:23040\n"
        "s_waitcnt lgkmcnt(7)\n"
        "v_mfma_f32_16x16x32_bf16 v[224:227], v[32:35], v[168:171], v[224:227]\n"
        "ds_read_b64_tr_b16 v[190:191], v233 offset:23184\n"
        "v_mfma_f32_16x16x32_bf16 v[228:231], v[36:39], v[168:171], v[228:231]\n"
        "s_barrier\n"
        "v_add_u32 v234, 24576, v234\n"
        "v_add_u32 v235, 24576, v235\n"
        "ds_read_b64_tr_b16 v[168:169], v234 offset:49152\n"
        "v_mfma_f32_16x16x32_bf16 v[192:195], v[40:43], v[172:175], v[192:195]\n"
        "ds_read_b64_tr_b16 v[170:171], v234 offset:49296\n"
        "v_mfma_f32_16x16x32_bf16 v[196:199], v[44:47], v[172:175], v[196:199]\n"
        "ds_read_b64_tr_b16 v[172:173], v234 offset:49192\n"
        "s_waitcnt lgkmcnt(7)\n"
        "v_mfma_f32_16x16x32_bf16 v[204:207], v[44:47], v[176:179], v[204:207]\n"
        "ds_read_b64_tr_b16 v[174:175], v234 offset:49336\n"
        "v_mfma_f32_16x16x32_bf16 v[200:203], v[40:43], v[176:179], v[200:203]\n"
        "ds_read_b64_tr_b16 v[176:177], v234 offset:49224\n"
        "v_mfma_f32_16x16x32_bf16 v[208:211], v[40:43], v[180:183], v[208:211]\n"
        "ds_read_b64_tr_b16 v[178:179], v234 offset:49368\n"
        "v_mfma_f32_16x16x32_bf16 v[212:215], v[44:47], v[180:183], v[212:215]\n"
        "ds_read_b64_tr_b16 v[180:181], v234 offset:49264\n"
        "s_waitcnt lgkmcnt(7)\n"
        "v_mfma_f32_16x16x32_bf16 v[220:223], v[44:47], v[184:187], v[220:223]\n"
        "ds_read_b64_tr_b16 v[182:183], v234 offset:49408\n"
        "v_mfma_f32_16x16x32_bf16 v[216:219], v[40:43], v[184:187], v[216:219]\n"
        "ds_read_b64_tr_b16 v[184:185], v235 offset:49152\n"
        "v_mfma_f32_16x16x32_bf16 v[224:227], v[40:43], v[188:191], v[224:227]\n"
        "ds_read_b64_tr_b16 v[186:187], v235 offset:49296\n"
        "v_mfma_f32_16x16x32_bf16 v[228:231], v[44:47], v[188:191], v[228:231]\n"
        "v_add_u32 v254, 98304, v238\n"
        "v_cvt_pk_bf16_f32 v192, v192, v193\n"
        "v_cvt_pk_bf16_f32 v193, v194, v195\n"
        "v_cvt_pk_bf16_f32 v194, v196, v197\n"
        "v_cvt_pk_bf16_f32 v195, v198, v199\n"
        "v_cvt_pk_bf16_f32 v200, v200, v201\n"
        "v_cvt_pk_bf16_f32 v201, v202, v203\n"
        "v_cvt_pk_bf16_f32 v202, v204, v205\n"
        "v_cvt_pk_bf16_f32 v203, v206, v207\n"
        "v_permlane32_swap_b32 v192, v194\n"
        "v_permlane32_swap_b32 v193, v195\n"
        "buffer_store_dwordx4 v[192:195], v238, s[24:27], 0 offen offset:1024 sc1\n"
        "ds_write_b128 v238, v[192:195] offset:17408\n"
        "v_cvt_pk_bf16_f32 v224, v224, v225\n"
        "v_cvt_pk_bf16_f32 v225, v226, v227\n"
        "v_cvt_pk_bf16_f32 v226, v228, v229\n"
        "v_cvt_pk_bf16_f32 v227, v230, v231\n"
        "v_permlane32_swap_b32 v200, v202\n"
        "v_permlane32_swap_b32 v201, v203\n"
        "ds_write_b128 v238, v[200:203] offset:41984\n"
        "v_cvt_pk_bf16_f32 v208, v208, v209\n"
        "v_cvt_pk_bf16_f32 v209, v210, v211\n"
        "v_cvt_pk_bf16_f32 v210, v212, v213\n"
        "v_cvt_pk_bf16_f32 v211, v214, v215\n"
        "v_permlane32_swap_b32 v224, v226\n"
        "v_permlane32_swap_b32 v225, v227\n"
        "v_cndmask_b32 v224, v224, v246, s[44:45]\n"
        "v_cndmask_b32 v225, v225, v246, s[44:45]\n"
        "v_cndmask_b32 v226, v226, v246, s[44:45]\n"
        "v_cndmask_b32 v227, v227, v246, s[44:45]\n"
        "buffer_store_dwordx4 v[224:227], v238, s[24:27], s51 offen offset:1024 sc1\n"
        "ds_write_b128 v254, v[224:227] offset:17408\n"
        "v_cvt_pk_bf16_f32 v216, v216, v217\n"
        "v_cvt_pk_bf16_f32 v217, v218, v219\n"
        "v_cvt_pk_bf16_f32 v218, v220, v221\n"
        "v_cvt_pk_bf16_f32 v219, v222, v223\n"
        "v_permlane32_swap_b32 v208, v210\n"
        "v_permlane32_swap_b32 v209, v211\n"
        "ds_write_b128 v239, v[208:211] offset:17408\n"
        "v_permlane32_swap_b32 v216, v218\n"
        "v_permlane32_swap_b32 v217, v219\n"
        "ds_write_b128 v239, v[216:219] offset:41984\n"
        "ds_read_b64_tr_b16 v[188:189], v234 offset:53760\n"
        "s_waitcnt lgkmcnt(12)\n"
        "v_mfma_f32_16x16x32_bf16 v[128:131], v[168:171], v[0:3], 0\n"
        "ds_read_b64_tr_b16 v[190:191], v234 offset:53904\n"
        "v_mfma_f32_16x16x32_bf16 v[132:135], v[168:171], v[4:7], 0\n"
        "ds_read_b64_tr_b16 v[168:169], v234 offset:53800\n"
        "v_mfma_f32_16x16x32_bf16 v[140:143], v[172:175], v[4:7], 0\n"
        "ds_read_b64_tr_b16 v[170:171], v234 offset:53944\n"
        "v_mfma_f32_16x16x32_bf16 v[136:139], v[172:175], v[0:3], 0\n"
        "ds_read_b64_tr_b16 v[172:173], v234 offset:53832\n"
        "s_waitcnt lgkmcnt(12)\n"
        "v_mfma_f32_16x16x32_bf16 v[144:147], v[176:179], v[0:3], 0\n"
        "ds_read_b64_tr_b16 v[174:175], v234 offset:53976\n"
        "v_mfma_f32_16x16x32_bf16 v[148:151], v[176:179], v[4:7], 0\n"
        "ds_read_b64_tr_b16 v[176:177], v234 offset:53872\n"
        "v_mfma_f32_16x16x32_bf16 v[156:159], v[180:183], v[4:7], 0\n"
        "ds_read_b64_tr_b16 v[178:179], v234 offset:54016\n"
        "v_mfma_f32_16x16x32_bf16 v[152:155], v[180:183], v[0:3], 0\n"
        "ds_read_b64_tr_b16 v[180:181], v235 offset:53760\n"
        "s_waitcnt lgkmcnt(7)\n"
        "v_mfma_f32_16x16x32_bf16 v[160:163], v[184:187], v[0:3], 0\n"
        "ds_read_b64_tr_b16 v[182:183], v235 offset:53904\n"
        "v_mfma_f32_16x16x32_bf16 v[164:167], v[184:187], v[4:7], 0\n"
        "s_barrier\n"
        "ds_read_b64_tr_b16 v[184:185], v232 offset:9216\n"
        "v_mfma_f32_16x16x32_bf16 v[128:131], v[188:191], v[8:11], v[128:131]\n"
        "ds_read_b64_tr_b16 v[186:187], v232 offset:9360\n"
        "v_mfma_f32_16x16x32_bf16 v[132:135], v[188:191], v[12:15], v[132:135]\n"
        "ds_read_b64_tr_b16 v[188:189], v232 offset:9256\n"
        "s_waitcnt lgkmcnt(7)\n"
        "v_mfma_f32_16x16x32_bf16 v[140:143], v[168:171], v[12:15], v[140:143]\n"
        "ds_read_b64_tr_b16 v[190:191], v232 offset:9400\n"
        "v_mfma_f32_16x16x32_bf16 v[136:139], v[168:171], v[8:11], v[136:139]\n"
        "ds_read_b64_tr_b16 v[168:169], v232 offset:9288\n"
        "v_mfma_f32_16x16x32_bf16 v[144:147], v[172:175], v[8:11], v[144:147]\n"
        "ds_read_b64_tr_b16 v[170:171], v232 offset:9432\n"
        "v_mfma_f32_16x16x32_bf16 v[148:151], v[172:175], v[12:15], v[148:151]\n"
        "ds_read_b64_tr_b16 v[172:173], v232 offset:9328\n"
        "s_waitcnt lgkmcnt(7)\n"
        "v_mfma_f32_16x16x32_bf16 v[156:159], v[176:179], v[12:15], v[156:159]\n"
        "ds_read_b64_tr_b16 v[174:175], v232 offset:9472\n"
        "v_mfma_f32_16x16x32_bf16 v[152:155], v[176:179], v[8:11], v[152:155]\n"
        "ds_read_b64_tr_b16 v[176:177], v233 offset:9216\n"
        "v_mfma_f32_16x16x32_bf16 v[160:163], v[180:183], v[8:11], v[160:163]\n"
        "ds_read_b64_tr_b16 v[178:179], v233 offset:9360\n"
        "v_mfma_f32_16x16x32_bf16 v[164:167], v[180:183], v[12:15], v[164:167]\n"
        "ds_read_b64_tr_b16 v[180:181], v232 offset:13824\n"
        "s_waitcnt lgkmcnt(7)\n"
        "v_mfma_f32_16x16x32_bf16 v[128:131], v[184:187], v[16:19], v[128:131]\n"
        "ds_read_b64_tr_b16 v[182:183], v232 offset:13968\n"
        "v_mfma_f32_16x16x32_bf16 v[132:135], v[184:187], v[20:23], v[132:135]\n"
        "ds_read_b128 v[0:3], v236 offset:0\n"
        "ds_read_b64_tr_b16 v[184:185], v232 offset:13864\n"
        "v_mfma_f32_16x16x32_bf16 v[140:143], v[188:191], v[20:23], v[140:143]\n"
        "ds_read_b64_tr_b16 v[186:187], v232 offset:14008\n"
        "v_mfma_f32_16x16x32_bf16 v[136:139], v[188:191], v[16:19], v[136:139]\n"
        "ds_read_b128 v[4:7], v236 offset:2048\n"
        "ds_read_b64_tr_b16 v[188:189], v232 offset:13896\n"
        "s_waitcnt lgkmcnt(9)\n"
        "v_mfma_f32_16x16x32_bf16 v[144:147], v[168:171], v[16:19], v[144:147]\n"
        "ds_read_b64_tr_b16 v[190:191], v232 offset:14040\n"
        "v_mfma_f32_16x16x32_bf16 v[148:151], v[168:171], v[20:23], v[148:151]\n"
        "ds_read_b128 v[8:11], v237 offset:0\n"
        "ds_read_b64_tr_b16 v[168:169], v232 offset:13936\n"
        "v_mfma_f32_16x16x32_bf16 v[156:159], v[172:175], v[20:23], v[156:159]\n"
        "ds_read_b64_tr_b16 v[170:171], v232 offset:14080\n"
        "v_mfma_f32_16x16x32_bf16 v[152:155], v[172:175], v[16:19], v[152:155]\n"
        "ds_read_b128 v[12:15], v237 offset:2048\n"
        "ds_read_b64_tr_b16 v[172:173], v233 offset:13824\n"
        "s_waitcnt lgkmcnt(11)\n"
        "v_mfma_f32_16x16x32_bf16 v[160:163], v[176:179], v[16:19], v[160:163]\n"
        "ds_read_b64_tr_b16 v[174:175], v233 offset:13968\n"
        "v_mfma_f32_16x16x32_bf16 v[164:167], v[176:179], v[20:23], v[164:167]\n"
        "s_barrier\n"
        "ds_read_b64_tr_b16 v[176:177], v232 offset:0\n"
        "v_mfma_f32_16x16x32_bf16 v[128:131], v[180:183], v[24:27], v[128:131]\n"
        "ds_read_b64_tr_b16 v[178:179], v232 offset:144\n"
        "v_mfma_f32_16x16x32_bf16 v[132:135], v[180:183], v[28:31], v[132:135]\n"
        "ds_read_b64_tr_b16 v[180:181], v232 offset:40\n"
        "s_waitcnt lgkmcnt(9)\n"
        "v_mfma_f32_16x16x32_bf16 v[140:143], v[184:187], v[28:31], v[140:143]\n"
        "ds_read_b64_tr_b16 v[182:183], v232 offset:184\n"
        "v_mfma_f32_16x16x32_bf16 v[136:139], v[184:187], v[24:27], v[136:139]\n"
        "ds_read_b64_tr_b16 v[184:185], v232 offset:72\n"
        "v_mfma_f32_16x16x32_bf16 v[144:147], v[188:191], v[24:27], v[144:147]\n"
        "ds_read_b64_tr_b16 v[186:187], v232 offset:216\n"
        "v_mfma_f32_16x16x32_bf16 v[148:151], v[188:191], v[28:31], v[148:151]\n"
        "ds_read_b64_tr_b16 v[188:189], v232 offset:112\n"
        "s_waitcnt lgkmcnt(7)\n"
        "v_mfma_f32_16x16x32_bf16 v[156:159], v[168:171], v[28:31], v[156:159]\n"
        "ds_read_b64_tr_b16 v[190:191], v232 offset:256\n"
        "v_mfma_f32_16x16x32_bf16 v[152:155], v[168:171], v[24:27], v[152:155]\n"
        "ds_read_b64_tr_b16 v[168:169], v233 offset:0\n"
        "v_mfma_f32_16x16x32_bf16 v[160:163], v[172:175], v[24:27], v[160:163]\n"
        "ds_read_b64_tr_b16 v[170:171], v233 offset:144\n"
        "v_mfma_f32_16x16x32_bf16 v[164:167], v[172:175], v[28:31], v[164:167]\n"
        "ds_read_b64_tr_b16 v[172:173], v232 offset:4608\n"
        "s_waitcnt lgkmcnt(7)\n"
        "v_mfma_f32_16x16x32_bf16 v[128:131], v[176:179], v[0:3], v[128:131]\n"
        "ds_read_b64_tr_b16 v[174:175], v232 offset:4752\n"
        "v_mfma_f32_16x16x32_bf16 v[132:135], v[176:179], v[4:7], v[132:135]\n"
        "ds_read_b64_tr_b16 v[176:177], v232 offset:4648\n"
        "v_mfma_f32_16x16x32_bf16 v[140:143], v[180:183], v[4:7], v[140:143]\n"
        "ds_read_b64_tr_b16 v[178:179], v232 offset:4792\n"
        "v_mfma_f32_16x16x32_bf16 v[136:139], v[180:183], v[0:3], v[136:139]\n"
        "ds_read_b64_tr_b16 v[180:181], v232 offset:4680\n"
        "s_waitcnt lgkmcnt(7)\n"
        "v_mfma_f32_16x16x32_bf16 v[144:147], v[184:187], v[0:3], v[144:147]\n"
        "ds_read_b64_tr_b16 v[182:183], v232 offset:4824\n"
        "v_mfma_f32_16x16x32_bf16 v[148:151], v[184:187], v[4:7], v[148:151]\n"
        "ds_read_b64_tr_b16 v[184:185], v232 offset:4720\n"
        "v_mfma_f32_16x16x32_bf16 v[156:159], v[188:191], v[4:7], v[156:159]\n"
        "ds_read_b64_tr_b16 v[186:187], v232 offset:4864\n"
        "v_mfma_f32_16x16x32_bf16 v[152:155], v[188:191], v[0:3], v[152:155]\n"
        "ds_read_b64_tr_b16 v[188:189], v233 offset:4608\n"
        "s_waitcnt lgkmcnt(7)\n"
        "v_mfma_f32_16x16x32_bf16 v[160:163], v[168:171], v[0:3], v[160:163]\n"
        "ds_read_b64_tr_b16 v[190:191], v233 offset:4752\n"
        "v_mfma_f32_16x16x32_bf16 v[164:167], v[168:171], v[4:7], v[164:167]\n"
        "s_waitcnt vmcnt(0)\n"
        "s_barrier\n"
        "ds_read_b64_tr_b16 v[168:169], v234 offset:24576\n"
        "v_mfma_f32_16x16x32_bf16 v[128:131], v[172:175], v[8:11], v[128:131]\n"
        "ds_read_b64_tr_b16 v[170:171], v234 offset:24720\n"
        "v_mfma_f32_16x16x32_bf16 v[132:135], v[172:175], v[12:15], v[132:135]\n"
        "ds_read_b64_tr_b16 v[172:173], v234 offset:24616\n"
        "s_waitcnt lgkmcnt(7)\n"
        "v_mfma_f32_16x16x32_bf16 v[140:143], v[176:179], v[12:15], v[140:143]\n"
        "ds_read_b64_tr_b16 v[174:175], v234 offset:24760\n"
        "v_mfma_f32_16x16x32_bf16 v[136:139], v[176:179], v[8:11], v[136:139]\n"
        "ds_read_b64_tr_b16 v[176:177], v234 offset:24648\n"
        "v_mfma_f32_16x16x32_bf16 v[144:147], v[180:183], v[8:11], v[144:147]\n"
        "ds_read_b64_tr_b16 v[178:179], v234 offset:24792\n"
        "v_mfma_f32_16x16x32_bf16 v[148:151], v[180:183], v[12:15], v[148:151]\n"
        "ds_read_b64_tr_b16 v[180:181], v234 offset:24688\n"
        "s_waitcnt lgkmcnt(7)\n"
        "v_mfma_f32_16x16x32_bf16 v[156:159], v[184:187], v[12:15], v[156:159]\n"
        "ds_read_b64_tr_b16 v[182:183], v234 offset:24832\n"
        "v_mfma_f32_16x16x32_bf16 v[152:155], v[184:187], v[8:11], v[152:155]\n"
        "ds_read_b64_tr_b16 v[184:185], v235 offset:24576\n"
        "v_mfma_f32_16x16x32_bf16 v[160:163], v[188:191], v[8:11], v[160:163]\n"
        "ds_read_b64_tr_b16 v[186:187], v235 offset:24720\n"
        "v_mfma_f32_16x16x32_bf16 v[164:167], v[188:191], v[12:15], v[164:167]\n"
        "ds_read_b64_tr_b16 v[188:189], v234 offset:29184\n"
        "s_waitcnt lgkmcnt(7)\n"
        "v_mfma_f32_16x16x32_bf16 v[128:131], v[168:171], v[48:51], v[128:131]\n"
        "ds_read_b64_tr_b16 v[190:191], v234 offset:29328\n"
        "v_mfma_f32_16x16x32_bf16 v[132:135], v[168:171], v[52:55], v[132:135]\n"
        "ds_read_b64_tr_b16 v[168:169], v234 offset:29224\n"
        "v_mfma_f32_16x16x32_bf16 v[140:143], v[172:175], v[52:55], v[140:143]\n"
        "ds_read_b64_tr_b16 v[170:171], v234 offset:29368\n"
        "v_mfma_f32_16x16x32_bf16 v[136:139], v[172:175], v[48:51], v[136:139]\n"
        "ds_read_b64_tr_b16 v[172:173], v234 offset:29256\n"
        "s_waitcnt lgkmcnt(7)\n"
        "v_mfma_f32_16x16x32_bf16 v[144:147], v[176:179], v[48:51], v[144:147]\n"
        "ds_read_b64_tr_b16 v[174:175], v234 offset:29400\n"
        "v_mfma_f32_16x16x32_bf16 v[148:151], v[176:179], v[52:55], v[148:151]\n"
        "ds_read_b64_tr_b16 v[176:177], v234 offset:29296\n"
        "v_mfma_f32_16x16x32_bf16 v[156:159], v[180:183], v[52:55], v[156:159]\n"
        "ds_read_b64_tr_b16 v[178:179], v234 offset:29440\n"
        "v_mfma_f32_16x16x32_bf16 v[152:155], v[180:183], v[48:51], v[152:155]\n"
        "ds_read_b64_tr_b16 v[180:181], v235 offset:29184\n"
        "s_waitcnt lgkmcnt(7)\n"
        "v_mfma_f32_16x16x32_bf16 v[160:163], v[184:187], v[48:51], v[160:163]\n"
        "ds_read_b64_tr_b16 v[182:183], v235 offset:29328\n"
        "v_mfma_f32_16x16x32_bf16 v[164:167], v[184:187], v[52:55], v[164:167]\n"
        "ds_read_b64_tr_b16 v[184:185], v232 offset:27648\n"
        "v_mfma_f32_16x16x32_bf16 v[128:131], v[188:191], v[56:59], v[128:131]\n"
        "ds_read_b64_tr_b16 v[186:187], v232 offset:27792\n"
        "v_mfma_f32_16x16x32_bf16 v[132:135], v[188:191], v[60:63], v[132:135]\n"
        "ds_read_b64_tr_b16 v[188:189], v232 offset:27688\n"
        "s_waitcnt lgkmcnt(7)\n"
        "v_mfma_f32_16x16x32_bf16 v[140:143], v[168:171], v[60:63], v[140:143]\n"
        "ds_read_b64_tr_b16 v[190:191], v232 offset:27832\n"
        "v_mfma_f32_16x16x32_bf16 v[136:139], v[168:171], v[56:59], v[136:139]\n"
        "ds_read_b64_tr_b16 v[168:169], v232 offset:27720\n"
        "v_mfma_f32_16x16x32_bf16 v[144:147], v[172:175], v[56:59], v[144:147]\n"
        "ds_read_b64_tr_b16 v[170:171], v232 offset:27864\n"
        "v_mfma_f32_16x16x32_bf16 v[148:151], v[172:175], v[60:63], v[148:151]\n"
        "ds_read_b64_tr_b16 v[172:173], v232 offset:27760\n"
        "s_waitcnt lgkmcnt(7)\n"
        "v_mfma_f32_16x16x32_bf16 v[156:159], v[176:179], v[60:63], v[156:159]\n"
        "ds_read_b64_tr_b16 v[174:175], v232 offset:27904\n"
        "v_mfma_f32_16x16x32_bf16 v[152:155], v[176:179], v[56:59], v[152:155]\n"
        "ds_read_b64_tr_b16 v[176:177], v233 offset:27648\n"
        "v_mfma_f32_16x16x32_bf16 v[160:163], v[180:183], v[56:59], v[160:163]\n"
        "ds_read_b64_tr_b16 v[178:179], v233 offset:27792\n"
        "v_mfma_f32_16x16x32_bf16 v[164:167], v[180:183], v[60:63], v[164:167]\n"
        "ds_read_b64_tr_b16 v[180:181], v232 offset:32256\n"
        "s_waitcnt lgkmcnt(7)\n"
        "v_mfma_f32_16x16x32_bf16 v[128:131], v[184:187], v[64:67], v[128:131]\n"
        "ds_read_b64_tr_b16 v[182:183], v232 offset:32400\n"
        "v_mfma_f32_16x16x32_bf16 v[132:135], v[184:187], v[68:71], v[132:135]\n"
        "ds_read_b64_tr_b16 v[184:185], v232 offset:32296\n"
        "v_mfma_f32_16x16x32_bf16 v[140:143], v[188:191], v[68:71], v[140:143]\n"
        "ds_read_b64_tr_b16 v[186:187], v232 offset:32440\n"
        "ds_read_b32 v236, v252\n"
        "v_mfma_f32_16x16x32_bf16 v[136:139], v[188:191], v[64:67], v[136:139]\n"
        "ds_read_b64_tr_b16 v[188:189], v232 offset:32328\n"
        "s_waitcnt lgkmcnt(8)\n"
        "v_mfma_f32_16x16x32_bf16 v[144:147], v[168:171], v[64:67], v[144:147]\n"
        "ds_read_b64_tr_b16 v[190:191], v232 offset:32472\n"
        "v_mfma_f32_16x16x32_bf16 v[148:151], v[168:171], v[68:71], v[148:151]\n"
        "ds_read_b64_tr_b16 v[168:169], v232 offset:32368\n"
        "v_mfma_f32_16x16x32_bf16 v[156:159], v[172:175], v[68:71], v[156:159]\n"
        "ds_read_b64_tr_b16 v[170:171], v232 offset:32512\n"
        "v_mfma_f32_16x16x32_bf16 v[152:155], v[172:175], v[64:67], v[152:155]\n"
        "ds_read_b64_tr_b16 v[172:173], v233 offset:32256\n"
        "s_waitcnt lgkmcnt(5)\n"
        "v_mfma_f32_16x16x32_bf16 v[160:163], v[176:179], v[64:67], v[160:163]\n"
        "ds_read_b64_tr_b16 v[174:175], v233 offset:32400\n"
        "v_cmp_gt_u32 vcc, 4, v236\n"
        "v_mfma_f32_16x16x32_bf16 v[164:167], v[176:179], v[68:71], v[164:167]\n"
        "s_cbranch_vccnz Lslow_115\n"
        "Lback_116:\n"
        "ds_read_b64_tr_b16 v[176:177], v232 offset:18432\n"
        "v_mfma_f32_16x16x32_bf16 v[128:131], v[180:183], v[72:75], v[128:131]\n"
        "ds_read_b64_tr_b16 v[178:179], v232 offset:18576\n"
        "v_mfma_f32_16x16x32_bf16 v[132:135], v[180:183], v[76:79], v[132:135]\n"
        "ds_read_b64_tr_b16 v[180:181], v232 offset:18472\n"
        "s_waitcnt lgkmcnt(7)\n"
        "v_mfma_f32_16x16x32_bf16 v[140:143], v[184:187], v[76:79], v[140:143]\n"
        "ds_read_b64_tr_b16 v[182:183], v232 offset:18616\n"
        "v_mfma_f32_16x16x32_bf16 v[136:139], v[184:187], v[72:75], v[136:139]\n"
        "ds_read_b64_tr_b16 v[184:185], v232 offset:18504\n"
        "v_mfma_f32_16x16x32_bf16 v[144:147], v[188:191], v[72:75], v[144:147]\n"
        "ds_read_b64_tr_b16 v[186:187], v232 offset:18648\n"
        "v_mfma_f32_16x16x32_bf16 v[148:151], v[188:191], v[76:79], v[148:151]\n"
        "ds_read_b64_tr_b16 v[188:189], v232 offset:18544\n"
        "s_waitcnt lgkmcnt(7)\n"
        "v_mfma_f32_16x16x32_bf16 v[156:159], v[168:171], v[76:79], v[156:159]\n"
        "ds_read_b64_tr_b16 v[190:191], v232 offset:18688\n"
        "v_mfma_f32_16x16x32_bf16 v[152:155], v[168:171], v[72:75], v[152:155]\n"
        "ds_read_b64_tr_b16 v[168:169], v233 offset:18432\n"
        "v_mfma_f32_16x16x32_bf16 v[160:163], v[172:175], v[72:75], v[160:163]\n"
        "ds_read_b64_tr_b16 v[170:171], v233 offset:18576\n"
        "v_mfma_f32_16x16x32_bf16 v[164:167], v[172:175], v[76:79], v[164:167]\n"
        "ds_read_b64_tr_b16 v[172:173], v232 offset:23040\n"
        "s_waitcnt lgkmcnt(7)\n"
        "v_mfma_f32_16x16x32_bf16 v[128:131], v[176:179], v[80:83], v[128:131]\n"
        "ds_read_b64_tr_b16 v[174:175], v232 offset:23184\n"
        "v_mfma_f32_16x16x32_bf16 v[132:135], v[176:179], v[84:87], v[132:135]\n"
        "ds_read_b64_tr_b16 v[176:177], v232 offset:23080\n"
        "v_mfma_f32_16x16x32_bf16 v[140:143], v[180:183], v[84:87], v[140:143]\n"
        "ds_read_b64_tr_b16 v[178:179], v232 offset:23224\n"
        "v_mfma_f32_16x16x32_bf16 v[136:139], v[180:183], v[80:83], v[136:139]\n"
        "ds_read_b64_tr_b16 v[180:181], v232 offset:23112\n"
        "s_waitcnt lgkmcnt(7)\n"
        "v_mfma_f32_16x16x32_bf16 v[144:147], v[184:187], v[80:83], v[144:147]\n"
        "ds_read_b64_tr_b16 v[182:183], v232 offset:23256\n"
        "v_mfma_f32_16x16x32_bf16 v[148:151], v[184:187], v[84:87], v[148:151]\n"
        "ds_read_b64_tr_b16 v[184:185], v232 offset:23152\n"
        "v_mfma_f32_16x16x32_bf16 v[156:159], v[188:191], v[84:87], v[156:159]\n"
        "ds_read_b64_tr_b16 v[186:187], v232 offset:23296\n"
        "v_mfma_f32_16x16x32_bf16 v[152:155], v[188:191], v[80:83], v[152:155]\n"
        "ds_read_b64_tr_b16 v[188:189], v233 offset:23040\n"
        "s_waitcnt lgkmcnt(7)\n"
        "v_mfma_f32_16x16x32_bf16 v[160:163], v[168:171], v[80:83], v[160:163]\n"
        "ds_read_b64_tr_b16 v[190:191], v233 offset:23184\n"
        "v_mfma_f32_16x16x32_bf16 v[164:167], v[168:171], v[84:87], v[164:167]\n"
        "ds_read_b64_tr_b16 v[168:169], v234 offset:49152\n"
        "v_mfma_f32_16x16x32_bf16 v[128:131], v[172:175], v[88:91], v[128:131]\n"
        "ds_read_b64_tr_b16 v[170:171], v234 offset:49296\n"
        "v_mfma_f32_16x16x32_bf16 v[132:135], v[172:175], v[92:95], v[132:135]\n"
        "ds_read_b64_tr_b16 v[172:173], v234 offset:49192\n"
        "s_waitcnt lgkmcnt(7)\n"
        "v_mfma_f32_16x16x32_bf16 v[140:143], v[176:179], v[92:95], v[140:143]\n"
        "ds_read_b64_tr_b16 v[174:175], v234 offset:49336\n"
        "v_mfma_f32_16x16x32_bf16 v[136:139], v[176:179], v[88:91], v[136:139]\n"
        "ds_read_b64_tr_b16 v[176:177], v234 offset:49224\n"
        "v_mfma_f32_16x16x32_bf16 v[144:147], v[180:183], v[88:91], v[144:147]\n"
        "ds_read_b64_tr_b16 v[178:179], v234 offset:49368\n"
        "v_mfma_f32_16x16x32_bf16 v[148:151], v[180:183], v[92:95], v[148:151]\n"
        "ds_read_b64_tr_b16 v[180:181], v234 offset:49264\n"
        "s_waitcnt lgkmcnt(7)\n"
        "v_mfma_f32_16x16x32_bf16 v[156:159], v[184:187], v[92:95], v[156:159]\n"
        "ds_read_b64_tr_b16 v[182:183], v234 offset:49408\n"
        "v_mfma_f32_16x16x32_bf16 v[152:155], v[184:187], v[88:91], v[152:155]\n"
        "ds_read_b64_tr_b16 v[184:185], v235 offset:49152\n"
        "v_mfma_f32_16x16x32_bf16 v[160:163], v[188:191], v[88:91], v[160:163]\n"
        "ds_read_b64_tr_b16 v[186:187], v235 offset:49296\n"
        "v_mfma_f32_16x16x32_bf16 v[164:167], v[188:191], v[92:95], v[164:167]\n"
        "ds_read_b64_tr_b16 v[188:189], v234 offset:53760\n"
        "s_waitcnt lgkmcnt(7)\n"
        "v_mfma_f32_16x16x32_bf16 v[128:131], v[168:171], v[96:99], v[128:131]\n"
        "ds_read_b64_tr_b16 v[190:191], v234 offset:53904\n"
        "v_mfma_f32_16x16x32_bf16 v[132:135], v[168:171], v[100:103], v[132:135]\n"
        "ds_read_b64_tr_b16 v[168:169], v234 offset:53800\n"
        "v_mfma_f32_16x16x32_bf16 v[140:143], v[172:175], v[100:103], v[140:143]\n"
        "ds_read_b64_tr_b16 v[170:171], v234 offset:53944\n"
        "ds_read_b32 v236, v252\n"
        "v_mfma_f32_16x16x32_bf16 v[136:139], v[172:175], v[96:99], v[136:139]\n"
        "ds_read_b64_tr_b16 v[172:173], v234 offset:53832\n"
        "s_waitcnt lgkmcnt(8)\n"
        "v_mfma_f32_16x16x32_bf16 v[144:147], v[176:179], v[96:99], v[144:147]\n"
        "ds_read_b64_tr_b16 v[174:175], v234 offset:53976\n"
        "v_mfma_f32_16x16x32_bf16 v[148:151], v[176:179], v[100:103], v[148:151]\n"
        "ds_read_b64_tr_b16 v[176:177], v234 offset:53872\n"
        "v_mfma_f32_16x16x32_bf16 v[156:159], v[180:183], v[100:103], v[156:159]\n"
        "ds_read_b64_tr_b16 v[178:179], v234 offset:54016\n"
        "v_mfma_f32_16x16x32_bf16 v[152:155], v[180:183], v[96:99], v[152:155]\n"
        "ds_read_b64_tr_b16 v[180:181], v235 offset:53760\n"
        "s_waitcnt lgkmcnt(5)\n"
        "v_mfma_f32_16x16x32_bf16 v[160:163], v[184:187], v[96:99], v[160:163]\n"
        "ds_read_b64_tr_b16 v[182:183], v235 offset:53904\n"
        "v_cmp_gt_u32 vcc, 6, v236\n"
        "v_mfma_f32_16x16x32_bf16 v[164:167], v[184:187], v[100:103], v[164:167]\n"
        "s_cbranch_vccnz Lslow_117\n"
        "Lback_118:\n"
        "ds_read_b64_tr_b16 v[184:185], v232 offset:9216\n"
        "v_mfma_f32_16x16x32_bf16 v[128:131], v[188:191], v[104:107], v[128:131]\n"
        "ds_read_b64_tr_b16 v[186:187], v232 offset:9360\n"
        "v_mfma_f32_16x16x32_bf16 v[132:135], v[188:191], v[108:111], v[132:135]\n"
        "ds_read_b64_tr_b16 v[188:189], v232 offset:9256\n"
        "s_waitcnt lgkmcnt(7)\n"
        "v_mfma_f32_16x16x32_bf16 v[140:143], v[168:171], v[108:111], v[140:143]\n"
        "ds_read_b64_tr_b16 v[190:191], v232 offset:9400\n"
        "v_mfma_f32_16x16x32_bf16 v[136:139], v[168:171], v[104:107], v[136:139]\n"
        "ds_read_b64_tr_b16 v[168:169], v232 offset:9288\n"
        "v_mfma_f32_16x16x32_bf16 v[144:147], v[172:175], v[104:107], v[144:147]\n"
        "ds_read_b64_tr_b16 v[170:171], v232 offset:9432\n"
        "v_mfma_f32_16x16x32_bf16 v[148:151], v[172:175], v[108:111], v[148:151]\n"
        "ds_read_b64_tr_b16 v[172:173], v232 offset:9328\n"
        "s_waitcnt lgkmcnt(7)\n"
        "v_mfma_f32_16x16x32_bf16 v[156:159], v[176:179], v[108:111], v[156:159]\n"
        "ds_read_b64_tr_b16 v[174:175], v232 offset:9472\n"
        "v_mfma_f32_16x16x32_bf16 v[152:155], v[176:179], v[104:107], v[152:155]\n"
        "ds_read_b64_tr_b16 v[176:177], v233 offset:9216\n"
        "v_mfma_f32_16x16x32_bf16 v[160:163], v[180:183], v[104:107], v[160:163]\n"
        "ds_read_b64_tr_b16 v[178:179], v233 offset:9360\n"
        "v_mfma_f32_16x16x32_bf16 v[164:167], v[180:183], v[108:111], v[164:167]\n"
        "ds_read_b64_tr_b16 v[180:181], v232 offset:13824\n"
        "s_waitcnt lgkmcnt(7)\n"
        "v_mfma_f32_16x16x32_bf16 v[128:131], v[184:187], v[112:115], v[128:131]\n"
        "ds_read_b64_tr_b16 v[182:183], v232 offset:13968\n"
        "v_mfma_f32_16x16x32_bf16 v[132:135], v[184:187], v[116:119], v[132:135]\n"
        "ds_read_b64_tr_b16 v[184:185], v232 offset:13864\n"
        "v_mfma_f32_16x16x32_bf16 v[140:143], v[188:191], v[116:119], v[140:143]\n"
        "ds_read_b64_tr_b16 v[186:187], v232 offset:14008\n"
        "v_mfma_f32_16x16x32_bf16 v[136:139], v[188:191], v[112:115], v[136:139]\n"
        "ds_read_b64_tr_b16 v[188:189], v232 offset:13896\n"
        "s_waitcnt lgkmcnt(7)\n"
        "v_mfma_f32_16x16x32_bf16 v[144:147], v[168:171], v[112:115], v[144:147]\n"
        "ds_read_b64_tr_b16 v[190:191], v232 offset:14040\n"
        "v_mfma_f32_16x16x32_bf16 v[148:151], v[168:171], v[116:119], v[148:151]\n"
        "ds_read_b64_tr_b16 v[168:169], v232 offset:13936\n"
        "v_mfma_f32_16x16x32_bf16 v[156:159], v[172:175], v[116:119], v[156:159]\n"
        "ds_read_b64_tr_b16 v[170:171], v232 offset:14080\n"
        "v_mfma_f32_16x16x32_bf16 v[152:155], v[172:175], v[112:115], v[152:155]\n"
        "ds_read_b64_tr_b16 v[172:173], v233 offset:13824\n"
        "s_waitcnt lgkmcnt(7)\n"
        "v_mfma_f32_16x16x32_bf16 v[160:163], v[176:179], v[112:115], v[160:163]\n"
        "ds_read_b64_tr_b16 v[174:175], v233 offset:13968\n"
        "v_mfma_f32_16x16x32_bf16 v[164:167], v[176:179], v[116:119], v[164:167]\n"
        "ds_read_b64_tr_b16 v[176:177], v234 offset:0\n"
        "v_mfma_f32_16x16x32_bf16 v[128:131], v[180:183], v[120:123], v[128:131]\n"
        "ds_read_b64_tr_b16 v[178:179], v234 offset:144\n"
        "v_mfma_f32_16x16x32_bf16 v[132:135], v[180:183], v[124:127], v[132:135]\n"
        "ds_read_b64_tr_b16 v[180:181], v234 offset:40\n"
        "s_waitcnt lgkmcnt(7)\n"
        "v_mfma_f32_16x16x32_bf16 v[140:143], v[184:187], v[124:127], v[140:143]\n"
        "ds_read_b64_tr_b16 v[182:183], v234 offset:184\n"
        "v_mfma_f32_16x16x32_bf16 v[136:139], v[184:187], v[120:123], v[136:139]\n"
        "ds_read_b64_tr_b16 v[184:185], v234 offset:72\n"
        "v_mfma_f32_16x16x32_bf16 v[144:147], v[188:191], v[120:123], v[144:147]\n"
        "ds_read_b64_tr_b16 v[186:187], v234 offset:216\n"
        "v_mfma_f32_16x16x32_bf16 v[148:151], v[188:191], v[124:127], v[148:151]\n"
        "ds_read_b64_tr_b16 v[188:189], v234 offset:112\n"
        "s_waitcnt lgkmcnt(7)\n"
        "v_mfma_f32_16x16x32_bf16 v[156:159], v[168:171], v[124:127], v[156:159]\n"
        "ds_read_b64_tr_b16 v[190:191], v234 offset:256\n"
        "v_mfma_f32_16x16x32_bf16 v[152:155], v[168:171], v[120:123], v[152:155]\n"
        "ds_read_b64_tr_b16 v[168:169], v235 offset:0\n"
        "v_mfma_f32_16x16x32_bf16 v[160:163], v[172:175], v[120:123], v[160:163]\n"
        "ds_read_b64_tr_b16 v[170:171], v235 offset:144\n"
        "v_mfma_f32_16x16x32_bf16 v[164:167], v[172:175], v[124:127], v[164:167]\n"
        "ds_read_b64_tr_b16 v[172:173], v234 offset:4608\n"
        "s_waitcnt lgkmcnt(7)\n"
        "v_mfma_f32_16x16x32_bf16 v[128:131], v[176:179], v[32:35], v[128:131]\n"
        "ds_read_b64_tr_b16 v[174:175], v234 offset:4752\n"
        "v_mfma_f32_16x16x32_bf16 v[132:135], v[176:179], v[36:39], v[132:135]\n"
        "ds_read_b64_tr_b16 v[176:177], v234 offset:4648\n"
        "v_mfma_f32_16x16x32_bf16 v[140:143], v[180:183], v[36:39], v[140:143]\n"
        "ds_read_b64_tr_b16 v[178:179], v234 offset:4792\n"
        "v_mfma_f32_16x16x32_bf16 v[136:139], v[180:183], v[32:35], v[136:139]\n"
        "ds_read_b64_tr_b16 v[180:181], v234 offset:4680\n"
        "s_waitcnt lgkmcnt(7)\n"
        "v_mfma_f32_16x16x32_bf16 v[144:147], v[184:187], v[32:35], v[144:147]\n"
        "ds_read_b64_tr_b16 v[182:183], v234 offset:4824\n"
        "v_mfma_f32_16x16x32_bf16 v[148:151], v[184:187], v[36:39], v[148:151]\n"
        "ds_read_b64_tr_b16 v[184:185], v234 offset:4720\n"
        "v_mov_b32 v237, 27\n"
        "v_mfma_f32_16x16x32_bf16 v[156:159], v[188:191], v[36:39], v[156:159]\n"
        "ds_read_b64_tr_b16 v[186:187], v234 offset:4864\n"
        "v_mfma_f32_16x16x32_bf16 v[152:155], v[188:191], v[32:35], v[152:155]\n"
        "ds_read_b64_tr_b16 v[188:189], v235 offset:4608\n"
        "s_waitcnt lgkmcnt(7)\n"
        "v_mfma_f32_16x16x32_bf16 v[160:163], v[168:171], v[32:35], v[160:163]\n"
        "ds_read_b64_tr_b16 v[190:191], v235 offset:4752\n"
        "v_mfma_f32_16x16x32_bf16 v[164:167], v[168:171], v[36:39], v[164:167]\n"
        "ds_read_b128 v[0:3], v242 offset:2048\n"
        "v_mfma_f32_16x16x32_bf16 v[128:131], v[172:175], v[40:43], v[128:131]\n"
        "ds_read_b128 v[4:7], v242 offset:6144\n"
        "v_mfma_f32_16x16x32_bf16 v[132:135], v[172:175], v[44:47], v[132:135]\n"
        "ds_read_b128 v[8:11], v242 offset:10240\n"
        "s_waitcnt lgkmcnt(7)\n"
        "v_mfma_f32_16x16x32_bf16 v[140:143], v[176:179], v[44:47], v[140:143]\n"
        "ds_read_b128 v[12:15], v242 offset:14336\n"
        "v_mfma_f32_16x16x32_bf16 v[136:139], v[176:179], v[40:43], v[136:139]\n"
        "ds_read_b128 v[16:19], v242 offset:18432\n"
        "v_mfma_f32_16x16x32_bf16 v[144:147], v[180:183], v[40:43], v[144:147]\n"
        "ds_read_b128 v[20:23], v242 offset:22528\n"
        "v_mfma_f32_16x16x32_bf16 v[148:151], v[180:183], v[44:47], v[148:151]\n"
        "ds_read_b128 v[24:27], v242 offset:63488\n"
        "s_waitcnt lgkmcnt(7)\n"
        "v_mfma_f32_16x16x32_bf16 v[156:159], v[184:187], v[44:47], v[156:159]\n"
        "ds_read_b128 v[28:31], v242 offset:64512\n"
        "v_mfma_f32_16x16x32_bf16 v[152:155], v[184:187], v[40:43], v[152:155]\n"
        "v_mfma_f32_16x16x32_bf16 v[160:163], v[188:191], v[40:43], v[160:163]\n"
        "ds_write_b32 v251, v237\n"
        "v_mfma_f32_16x16x32_bf16 v[164:167], v[188:191], v[44:47], v[164:167]\n"
        "ds_read_b128 v[116:119], v238 offset:16384\n"
        "ds_read_b64 v[112:113], v240 offset:16384\n"
        "ds_read_b128 v[120:123], v247 offset:16384\n"
        "ds_read_b128 v[124:127], v248 offset:16384\n"
        "v_cvt_pk_bf16_f32 v208, v128, v129\n"
        "v_cvt_pk_bf16_f32 v209, v130, v131\n"
        "v_cvt_pk_bf16_f32 v210, v132, v133\n"
        "v_cvt_pk_bf16_f32 v211, v134, v135\n"
        "v_mov_b32 v49, 0\n"
        "v_mov_b32 v50, 0\n"
        "v_permlane16_swap_b32 v208, v210\n"
        "v_permlane16_swap_b32 v209, v211\n"
        "v_cvt_pk_bf16_f32 v48, v160, v161\n"
        "v_cvt_pk_bf16_f32 v51, v162, v163\n"
        "v_cvt_pk_bf16_f32 v53, v164, v165\n"
        "v_cvt_pk_bf16_f32 v54, v166, v167\n"
        "s_waitcnt lgkmcnt(2)\n"
        "v_mfma_f32_32x32x16_bf16 v[64:79], v[116:119], v[208:211], v[0:15]\n"
        "ds_read_b128 v[116:119], v238 offset:40960\n"
        "v_permlane16_swap_b32 v48, v53\n"
        "v_permlane16_swap_b32 v51, v54\n"
        "v_cvt_pk_bf16_f32 v212, v136, v137\n"
        "v_cvt_pk_bf16_f32 v213, v138, v139\n"
        "v_cvt_pk_bf16_f32 v214, v140, v141\n"
        "v_cvt_pk_bf16_f32 v215, v142, v143\n"
        "v_mfma_f32_32x32x8_bf16 v[64:79], v[112:113], v[48:49], v[64:79]\n"
        "v_mov_b32 v52, 0\n"
        "v_mov_b32 v55, 0\n"
        "v_cvt_pk_bf16_f32 v216, v144, v145\n"
        "v_cvt_pk_bf16_f32 v217, v146, v147\n"
        "v_cvt_pk_bf16_f32 v218, v148, v149\n"
        "v_cvt_pk_bf16_f32 v219, v150, v151\n"
        "v_permlane16_swap_b32 v212, v214\n"
        "v_permlane16_swap_b32 v213, v215\n"
        "v_cvt_pk_bf16_f32 v220, v152, v153\n"
        "v_cvt_pk_bf16_f32 v221, v154, v155\n"
        "v_cvt_pk_bf16_f32 v222, v156, v157\n"
        "v_cvt_pk_bf16_f32 v223, v158, v159\n"
        "v_permlane16_swap_b32 v216, v218\n"
        "v_permlane16_swap_b32 v217, v219\n"
        "v_permlane32_swap_b32 v52, v53\n"
        "v_permlane32_swap_b32 v55, v54\n"
        "v_permlane16_swap_b32 v220, v222\n"
        "v_permlane16_swap_b32 v221, v223\n"
        "v_exp_f32 v64, v64\n"
        "v_exp_f32 v65, v65\n"
        "ds_read_b128 v[168:171], v247 offset:16896\n"
        "v_exp_f32 v66, v66\n"
        "v_exp_f32 v67, v67\n"
        "v_cvt_pk_bf16_f32 v128, v64, v65\n"
        "v_exp_f32 v68, v68\n"
        "v_exp_f32 v69, v69\n"
        "s_waitcnt lgkmcnt(1)\n"
        "v_mfma_f32_32x32x16_bf16 v[80:95], v[116:119], v[212:215], v[0:15]\n"
        "ds_read_b128 v[116:119], v239 offset:16384\n"
        "v_exp_f32 v70, v70\n"
        "v_cvt_pk_bf16_f32 v129, v66, v67\n"
        "v_exp_f32 v71, v71\n"
        "v_cvt_pk_bf16_f32 v130, v68, v69\n"
        "v_exp_f32 v72, v72\n"
        "v_cvt_pk_bf16_f32 v131, v70, v71\n"
        "v_exp_f32 v73, v73\n"
        "v_mfma_f32_32x32x8_bf16 v[80:95], v[112:113], v[50:51], v[80:95]\n"
        "v_exp_f32 v74, v74\n"
        "v_exp_f32 v75, v75\n"
        "v_cvt_pk_bf16_f32 v132, v72, v73\n"
        "v_exp_f32 v76, v76\n"
        "v_exp_f32 v77, v77\n"
        "v_mfma_f32_32x32x16_bf16 v[136:151], v[120:123], v[128:131], 0\n"
        "ds_read_b128 v[120:123], v249 offset:16384\n"
        "v_exp_f32 v78, v78\n"
        "v_cvt_pk_bf16_f32 v133, v74, v75\n"
        "v_exp_f32 v79, v79\n"
        "v_cvt_pk_bf16_f32 v134, v76, v77\n"
        "v_cvt_pk_bf16_f32 v135, v78, v79\n"
        "v_exp_f32 v80, v80\n"
        "v_exp_f32 v81, v81\n"
        "s_waitcnt lgkmcnt(1)\n"
        "v_mfma_f32_32x32x16_bf16 v[136:151], v[168:171], v[132:135], v[136:151]\n"
        "ds_read_b128 v[168:171], v248 offset:16896\n"
        "ds_read_b32 v236, v252\n"
        "v_exp_f32 v82, v82\n"
        "v_exp_f32 v83, v83\n"
        "v_cvt_pk_bf16_f32 v128, v80, v81\n"
        "v_exp_f32 v84, v84\n"
        "v_exp_f32 v85, v85\n"
        "v_mfma_f32_32x32x16_bf16 v[64:79], v[116:119], v[216:219], v[0:15]\n"
        "v_exp_f32 v86, v86\n"
        "v_cvt_pk_bf16_f32 v129, v82, v83\n"
        "v_exp_f32 v87, v87\n"
        "v_cvt_pk_bf16_f32 v130, v84, v85\n"
        "v_exp_f32 v88, v88\n"
        "v_cvt_pk_bf16_f32 v131, v86, v87\n"
        "v_exp_f32 v89, v89\n"
        "v_mfma_f32_32x32x8_bf16 v[64:79], v[112:113], v[52:53], v[64:79]\n"
        "v_exp_f32 v90, v90\n"
        "v_exp_f32 v91, v91\n"
        "v_cvt_pk_bf16_f32 v132, v88, v89\n"
        "v_exp_f32 v92, v92\n"
        "v_exp_f32 v93, v93\n"
        "s_waitcnt lgkmcnt(0)\n"
        "v_cmp_gt_u32 vcc, 7, v236\n"
        "v_mfma_f32_32x32x16_bf16 v[152:167], v[124:127], v[128:131], 0\n"
        "s_cbranch_vccnz Lslow_119\n"
        "Lback_120:\n"
        "ds_read_b128 v[116:119], v238 offset:14336\n"
        "ds_read_b64 v[110:111], v241 offset:12288\n"
        "ds_read_b128 v[124:127], v247 offset:14336\n"
        "ds_read_b64 v[108:109], v240 offset:12288\n"
        "ds_read_b64 v[106:107], v241 offset:8192\n"
        "ds_read_b64 v[104:105], v240 offset:8192\n"
        "ds_read_b64 v[102:103], v241 offset:4096\n"
        "ds_read_b64 v[100:101], v240 offset:4096\n"
        "ds_read_b64 v[98:99], v241 offset:0\n"
        "ds_read_b64 v[96:97], v240 offset:0\n"
        "v_exp_f32 v94, v94\n"
        "v_cvt_pk_bf16_f32 v133, v90, v91\n"
        "v_exp_f32 v95, v95\n"
        "v_cvt_pk_bf16_f32 v134, v92, v93\n"
        "v_cvt_pk_bf16_f32 v135, v94, v95\n"
        "v_exp_f32 v64, v64\n"
        "v_exp_f32 v65, v65\n"
        "v_mfma_f32_32x32x16_bf16 v[152:167], v[168:171], v[132:135], v[152:167]\n"
        "ds_read_b128 v[168:171], v249 offset:16896\n"
        "v_exp_f32 v66, v66\n"
        "v_exp_f32 v67, v67\n"
        "v_cvt_pk_bf16_f32 v128, v64, v65\n"
        "v_exp_f32 v68, v68\n"
        "v_exp_f32 v69, v69\n"
        "s_waitcnt lgkmcnt(10)\n"
        "v_mfma_f32_32x32x16_bf16 v[80:95], v[116:119], v[208:211], 0\n"
        "ds_read_b128 v[116:119], v238 offset:12288\n"
        "v_exp_f32 v70, v70\n"
        "v_cvt_pk_bf16_f32 v129, v66, v67\n"
        "v_exp_f32 v71, v71\n"
        "v_cvt_pk_bf16_f32 v130, v68, v69\n"
        "v_exp_f32 v72, v72\n"
        "v_cvt_pk_bf16_f32 v131, v70, v71\n"
        "v_exp_f32 v73, v73\n"
        "s_waitcnt lgkmcnt(10)\n"
        "v_mfma_f32_32x32x8_bf16 v[80:95], v[110:111], v[48:49], v[80:95]\n"
        "v_exp_f32 v74, v74\n"
        "v_exp_f32 v75, v75\n"
        "v_cvt_pk_bf16_f32 v132, v72, v73\n"
        "v_exp_f32 v76, v76\n"
        "v_exp_f32 v77, v77\n"
        "v_mfma_f32_32x32x16_bf16 v[32:47], v[120:123], v[128:131], 0\n"
        "ds_read_b128 v[120:123], v247 offset:12288\n"
        "v_exp_f32 v78, v78\n"
        "v_cvt_pk_bf16_f32 v133, v74, v75\n"
        "v_exp_f32 v79, v79\n"
        "v_cvt_pk_bf16_f32 v134, v76, v77\n"
        "v_cvt_pk_bf16_f32 v135, v78, v79\n"
        "v_exp_f32 v80, v80\n"
        "v_exp_f32 v81, v81\n"
        "s_waitcnt lgkmcnt(1)\n"
        "v_mfma_f32_32x32x16_bf16 v[32:47], v[168:171], v[132:135], v[32:47]\n"
        "ds_read_b128 v[168:171], v247 offset:14848\n"
        "v_exp_f32 v82, v82\n"
        "v_exp_f32 v83, v83\n"
        "v_cvt_pk_bf16_f32 v128, v80, v81\n"
        "v_exp_f32 v84, v84\n"
        "v_exp_f32 v85, v85\n"
        "v_mfma_f32_32x32x16_bf16 v[64:79], v[116:119], v[208:211], 0\n"
        "ds_read_b128 v[116:119], v238 offset:10240\n"
        "v_exp_f32 v86, v86\n"
        "v_cvt_pk_bf16_f32 v129, v82, v83\n"
        "v_exp_f32 v87, v87\n"
        "v_cvt_pk_bf16_f32 v130, v84, v85\n"
        "v_exp_f32 v88, v88\n"
        "v_cvt_pk_bf16_f32 v131, v86, v87\n"
        "v_exp_f32 v89, v89\n"
        "v_mfma_f32_32x32x8_bf16 v[64:79], v[108:109], v[48:49], v[64:79]\n"
        "v_exp_f32 v90, v90\n"
        "v_exp_f32 v91, v91\n"
        "v_cvt_pk_bf16_f32 v132, v88, v89\n"
        "v_exp_f32 v92, v92\n"
        "v_exp_f32 v93, v93\n"
        "v_mfma_f32_32x32x16_bf16 v[136:151], v[124:127], v[128:131], v[136:151]\n"
        "ds_read_b128 v[124:127], v247 offset:10240\n"
        "v_exp_f32 v94, v94\n"
        "v_cvt_pk_bf16_f32 v133, v90, v91\n"
        "v_exp_f32 v95, v95\n"
        "v_cvt_pk_bf16_f32 v134, v92, v93\n"
        "v_cvt_pk_bf16_f32 v135, v94, v95\n"
        "v_exp_f32 v64, v64\n"
        "v_exp_f32 v65, v65\n"
        "s_waitcnt lgkmcnt(1)\n"
        "v_mfma_f32_32x32x16_bf16 v[136:151], v[168:171], v[132:135], v[136:151]\n"
        "ds_read_b128 v[168:171], v247 offset:12800\n"
        "v_exp_f32 v66, v66\n"
        "v_exp_f32 v67, v67\n"
        "v_cvt_pk_bf16_f32 v128, v64, v65\n"
        "v_exp_f32 v68, v68\n"
        "v_exp_f32 v69, v69\n"
        "v_mfma_f32_32x32x16_bf16 v[80:95], v[116:119], v[208:211], 0\n"
        "ds_read_b128 v[116:119], v238 offset:8192\n"
        "v_exp_f32 v70, v70\n"
        "v_cvt_pk_bf16_f32 v129, v66, v67\n"
        "v_exp_f32 v71, v71\n"
        "v_cvt_pk_bf16_f32 v130, v68, v69\n"
        "v_exp_f32 v72, v72\n"
        "v_cvt_pk_bf16_f32 v131, v70, v71\n"
        "v_exp_f32 v73, v73\n"
        "v_mfma_f32_32x32x8_bf16 v[80:95], v[106:107], v[48:49], v[80:95]\n"
        "v_exp_f32 v74, v74\n"
        "v_exp_f32 v75, v75\n"
        "v_cvt_pk_bf16_f32 v132, v72, v73\n"
        "v_exp_f32 v76, v76\n"
        "v_exp_f32 v77, v77\n"
        "v_mfma_f32_32x32x16_bf16 v[136:151], v[120:123], v[128:131], v[136:151]\n"
        "ds_read_b128 v[120:123], v247 offset:8192\n"
        "v_exp_f32 v78, v78\n"
        "v_cvt_pk_bf16_f32 v133, v74, v75\n"
        "v_exp_f32 v79, v79\n"
        "v_cvt_pk_bf16_f32 v134, v76, v77\n"
        "v_cvt_pk_bf16_f32 v135, v78, v79\n"
        "v_exp_f32 v80, v80\n"
        "v_exp_f32 v81, v81\n"
        "s_waitcnt lgkmcnt(1)\n"
        "v_mfma_f32_32x32x16_bf16 v[136:151], v[168:171], v[132:135], v[136:151]\n"
        "ds_read_b128 v[168:171], v247 offset:10752\n"
        "v_exp_f32 v82, v82\n"
        "v_exp_f32 v83, v83\n"
        "v_cvt_pk_bf16_f32 v128, v80, v81\n"
        "v_exp_f32 v84, v84\n"
        "v_exp_f32 v85, v85\n"
        "v_mfma_f32_32x32x16_bf16 v[64:79], v[116:119], v[208:211], 0\n"
        "ds_read_b128 v[116:119], v238 offset:6144\n"
        "v_exp_f32 v86, v86\n"
        "v_cvt_pk_bf16_f32 v129, v82, v83\n"
        "v_exp_f32 v87, v87\n"
        "v_cvt_pk_bf16_f32 v130, v84, v85\n"
        "v_exp_f32 v88, v88\n"
        "v_cvt_pk_bf16_f32 v131, v86, v87\n"
        "v_exp_f32 v89, v89\n"
        "v_mfma_f32_32x32x8_bf16 v[64:79], v[104:105], v[48:49], v[64:79]\n"
        "v_exp_f32 v90, v90\n"
        "v_exp_f32 v91, v91\n"
        "v_cvt_pk_bf16_f32 v132, v88, v89\n"
        "v_exp_f32 v92, v92\n"
        "v_exp_f32 v93, v93\n"
        "v_mfma_f32_32x32x16_bf16 v[136:151], v[124:127], v[128:131], v[136:151]\n"
        "ds_read_b128 v[124:127], v247 offset:6144\n"
        "v_exp_f32 v94, v94\n"
        "v_cvt_pk_bf16_f32 v133, v90, v91\n"
        "v_exp_f32 v95, v95\n"
        "v_cvt_pk_bf16_f32 v134, v92, v93\n"
        "v_cvt_pk_bf16_f32 v135, v94, v95\n"
        "v_exp_f32 v64, v64\n"
        "v_exp_f32 v65, v65\n"
        "s_waitcnt lgkmcnt(1)\n"
        "v_mfma_f32_32x32x16_bf16 v[136:151], v[168:171], v[132:135], v[136:151]\n"
        "ds_read_b128 v[168:171], v247 offset:8704\n"
        "v_exp_f32 v66, v66\n"
        "v_exp_f32 v67, v67\n"
        "v_cvt_pk_bf16_f32 v128, v64, v65\n"
        "v_exp_f32 v68, v68\n"
        "v_exp_f32 v69, v69\n"
        "v_mfma_f32_32x32x16_bf16 v[80:95], v[116:119], v[208:211], 0\n"
        "ds_read_b128 v[116:119], v238 offset:4096\n"
        "v_exp_f32 v70, v70\n"
        "v_cvt_pk_bf16_f32 v129, v66, v67\n"
        "v_exp_f32 v71, v71\n"
        "v_cvt_pk_bf16_f32 v130, v68, v69\n"
        "v_exp_f32 v72, v72\n"
        "v_cvt_pk_bf16_f32 v131, v70, v71\n"
        "v_exp_f32 v73, v73\n"
        "v_mfma_f32_32x32x8_bf16 v[80:95], v[102:103], v[48:49], v[80:95]\n"
        "v_exp_f32 v74, v74\n"
        "v_exp_f32 v75, v75\n"
        "v_cvt_pk_bf16_f32 v132, v72, v73\n"
        "v_exp_f32 v76, v76\n"
        "v_exp_f32 v77, v77\n"
        "v_mfma_f32_32x32x16_bf16 v[136:151], v[120:123], v[128:131], v[136:151]\n"
        "ds_read_b128 v[120:123], v247 offset:4096\n"
        "v_exp_f32 v78, v78\n"
        "v_cvt_pk_bf16_f32 v133, v74, v75\n"
        "v_exp_f32 v79, v79\n"
        "v_cvt_pk_bf16_f32 v134, v76, v77\n"
        "v_cvt_pk_bf16_f32 v135, v78, v79\n"
        "v_exp_f32 v80, v80\n"
        "v_exp_f32 v81, v81\n"
        "s_waitcnt lgkmcnt(1)\n"
        "v_mfma_f32_32x32x16_bf16 v[136:151], v[168:171], v[132:135], v[136:151]\n"
        "ds_read_b128 v[168:171], v247 offset:6656\n"
        "v_exp_f32 v82, v82\n"
        "v_exp_f32 v83, v83\n"
        "v_cvt_pk_bf16_f32 v128, v80, v81\n"
        "v_exp_f32 v84, v84\n"
        "v_exp_f32 v85, v85\n"
        "v_mfma_f32_32x32x16_bf16 v[64:79], v[116:119], v[208:211], 0\n"
        "ds_read_b128 v[116:119], v238 offset:2048\n"
        "v_exp_f32 v86, v86\n"
        "v_cvt_pk_bf16_f32 v129, v82, v83\n"
        "v_exp_f32 v87, v87\n"
        "v_cvt_pk_bf16_f32 v130, v84, v85\n"
        "v_exp_f32 v88, v88\n"
        "v_cvt_pk_bf16_f32 v131, v86, v87\n"
        "v_exp_f32 v89, v89\n"
        "v_mfma_f32_32x32x8_bf16 v[64:79], v[100:101], v[48:49], v[64:79]\n"
        "v_exp_f32 v90, v90\n"
        "v_exp_f32 v91, v91\n"
        "v_cvt_pk_bf16_f32 v132, v88, v89\n"
        "v_exp_f32 v92, v92\n"
        "v_exp_f32 v93, v93\n"
        "v_mfma_f32_32x32x16_bf16 v[136:151], v[124:127], v[128:131], v[136:151]\n"
        "ds_read_b128 v[124:127], v247 offset:2048\n"
        "v_exp_f32 v94, v94\n"
        "v_cvt_pk_bf16_f32 v133, v90, v91\n"
        "v_exp_f32 v95, v95\n"
        "v_cvt_pk_bf16_f32 v134, v92, v93\n"
        "v_cvt_pk_bf16_f32 v135, v94, v95\n"
        "v_exp_f32 v64, v64\n"
        "v_exp_f32 v65, v65\n"
        "s_waitcnt lgkmcnt(1)\n"
        "v_mfma_f32_32x32x16_bf16 v[136:151], v[168:171], v[132:135], v[136:151]\n"
        "ds_read_b128 v[168:171], v247 offset:4608\n"
        "v_exp_f32 v66, v66\n"
        "v_exp_f32 v67, v67\n"
        "v_cvt_pk_bf16_f32 v128, v64, v65\n"
        "v_exp_f32 v68, v68\n"
        "v_exp_f32 v69, v69\n"
        "v_mfma_f32_32x32x16_bf16 v[80:95], v[116:119], v[208:211], 0\n"
        "ds_read_b128 v[116:119], v238 offset:0\n"
        "v_exp_f32 v70, v70\n"
        "v_cvt_pk_bf16_f32 v129, v66, v67\n"
        "v_exp_f32 v71, v71\n"
        "v_cvt_pk_bf16_f32 v130, v68, v69\n"
        "v_exp_f32 v72, v72\n"
        "v_cvt_pk_bf16_f32 v131, v70, v71\n"
        "v_exp_f32 v73, v73\n"
        "v_mfma_f32_32x32x8_bf16 v[80:95], v[98:99], v[48:49], v[80:95]\n"
        "v_exp_f32 v74, v74\n"
        "v_exp_f32 v75, v75\n"
        "v_cvt_pk_bf16_f32 v132, v72, v73\n"
        "v_exp_f32 v76, v76\n"
        "v_exp_f32 v77, v77\n"
        "v_mfma_f32_32x32x16_bf16 v[136:151], v[120:123], v[128:131], v[136:151]\n"
        "ds_read_b128 v[120:123], v247 offset:0\n"
        "v_exp_f32 v78, v78\n"
        "v_cvt_pk_bf16_f32 v133, v74, v75\n"
        "v_exp_f32 v79, v79\n"
        "v_cvt_pk_bf16_f32 v134, v76, v77\n"
        "v_cvt_pk_bf16_f32 v135, v78, v79\n"
        "v_exp_f32 v80, v80\n"
        "v_exp_f32 v81, v81\n"
        "s_waitcnt lgkmcnt(1)\n"
        "v_mfma_f32_32x32x16_bf16 v[136:151], v[168:171], v[132:135], v[136:151]\n"
        "ds_read_b128 v[168:171], v247 offset:2560\n"
        "ds_read_b32 v236, v252\n"
        "v_exp_f32 v82, v82\n"
        "v_exp_f32 v83, v83\n"
        "v_cvt_pk_bf16_f32 v128, v80, v81\n"
        "v_exp_f32 v84, v84\n"
        "v_exp_f32 v85, v85\n"
        "v_mfma_f32_32x32x16_bf16 v[64:79], v[116:119], v[208:211], v[16:31]\n"
        "v_exp_f32 v86, v86\n"
        "v_cvt_pk_bf16_f32 v129, v82, v83\n"
        "v_exp_f32 v87, v87\n"
        "v_cvt_pk_bf16_f32 v130, v84, v85\n"
        "v_exp_f32 v88, v88\n"
        "v_cvt_pk_bf16_f32 v131, v86, v87\n"
        "v_exp_f32 v89, v89\n"
        "v_mfma_f32_32x32x8_bf16 v[64:79], v[96:97], v[48:49], v[64:79]\n"
        "v_exp_f32 v90, v90\n"
        "v_exp_f32 v91, v91\n"
        "v_cvt_pk_bf16_f32 v132, v88, v89\n"
        "v_exp_f32 v92, v92\n"
        "v_exp_f32 v93, v93\n"
        "s_waitcnt lgkmcnt(0)\n"
        "v_cmp_gt_u32 vcc, 8, v236\n"
        "v_mfma_f32_32x32x16_bf16 v[136:151], v[124:127], v[128:131], v[136:151]\n"
        "s_cbranch_vccnz Lslow_121\n"
        "Lback_122:\n"
        "ds_read_b128 v[116:119], v238 offset:38912\n"
        "ds_read_b128 v[124:127], v248 offset:14336\n"
        "v_exp_f32 v94, v94\n"
        "v_cvt_pk_bf16_f32 v133, v90, v91\n"
        "v_exp_f32 v95, v95\n"
        "v_cvt_pk_bf16_f32 v134, v92, v93\n"
        "v_cvt_pk_bf16_f32 v135, v94, v95\n"
        "v_exp_f32 v64, v64\n"
        "v_exp_f32 v65, v65\n"
        "v_mfma_f32_32x32x16_bf16 v[136:151], v[168:171], v[132:135], v[136:151]\n"
        "ds_read_b128 v[168:171], v247 offset:512\n"
        "v_exp_f32 v66, v66\n"
        "v_exp_f32 v67, v67\n"
        "v_cvt_pk_bf16_f32 v128, v64, v65\n"
        "v_exp_f32 v68, v68\n"
        "v_exp_f32 v69, v69\n"
        "s_waitcnt lgkmcnt(2)\n"
        "v_mfma_f32_32x32x16_bf16 v[80:95], v[116:119], v[212:215], 0\n"
        "ds_read_b128 v[116:119], v238 offset:36864\n"
        "v_exp_f32 v70, v70\n"
        "v_cvt_pk_bf16_f32 v129, v66, v67\n"
        "v_exp_f32 v71, v71\n"
        "v_cvt_pk_bf16_f32 v130, v68, v69\n"
        "v_exp_f32 v72, v72\n"
        "v_cvt_pk_bf16_f32 v131, v70, v71\n"
        "v_exp_f32 v73, v73\n"
        "v_mfma_f32_32x32x8_bf16 v[80:95], v[110:111], v[50:51], v[80:95]\n"
        "v_exp_f32 v74, v74\n"
        "v_exp_f32 v75, v75\n"
        "v_cvt_pk_bf16_f32 v132, v72, v73\n"
        "v_exp_f32 v76, v76\n"
        "v_exp_f32 v77, v77\n"
        "v_mfma_f32_32x32x16_bf16 v[136:151], v[120:123], v[128:131], v[136:151]\n"
        "ds_read_b128 v[120:123], v248 offset:12288\n"
        "v_exp_f32 v78, v78\n"
        "v_cvt_pk_bf16_f32 v133, v74, v75\n"
        "v_exp_f32 v79, v79\n"
        "v_cvt_pk_bf16_f32 v134, v76, v77\n"
        "v_cvt_pk_bf16_f32 v135, v78, v79\n"
        "v_exp_f32 v80, v80\n"
        "v_exp_f32 v81, v81\n"
        "s_waitcnt lgkmcnt(1)\n"
        "v_mfma_f32_32x32x16_bf16 v[136:151], v[168:171], v[132:135], v[136:151]\n"
        "ds_read_b128 v[168:171], v248 offset:14848\n"
        "v_exp_f32 v82, v82\n"
        "v_exp_f32 v83, v83\n"
        "v_cvt_pk_bf16_f32 v128, v80, v81\n"
        "v_exp_f32 v84, v84\n"
        "v_exp_f32 v85, v85\n"
        "v_mfma_f32_32x32x16_bf16 v[64:79], v[116:119], v[212:215], 0\n"
        "ds_read_b128 v[116:119], v238 offset:34816\n"
        "v_exp_f32 v86, v86\n"
        "v_cvt_pk_bf16_f32 v129, v82, v83\n"
        "v_exp_f32 v87, v87\n"
        "v_cvt_pk_bf16_f32 v130, v84, v85\n"
        "v_exp_f32 v88, v88\n"
        "v_cvt_pk_bf16_f32 v131, v86, v87\n"
        "v_exp_f32 v89, v89\n"
        "v_mfma_f32_32x32x8_bf16 v[64:79], v[108:109], v[50:51], v[64:79]\n"
        "v_rcp_f32 v58, v148\n"
        "v_maximum3_f32 v57, |v136|, |v137|, v148\n"
        "v_maximum3_f32 v57, |v138|, |v139|, v57\n"
        "v_maximum3_f32 v57, |v140|, |v141|, v57\n"
        "v_maximum3_f32 v57, |v142|, |v143|, v57\n"
        "v_maximum3_f32 v57, |v144|, |v145|, v57\n"
        "v_maximum3_f32 v57, v58, v57, v57\n"
        "v_cmp_ngt_f32 vcc, s62, v57\n"
        "v_pk_mul_f32 v[136:137], v[136:137], v[58:59] op_sel_hi:[1,0]\n"
        "v_pk_mul_f32 v[138:139], v[138:139], v[58:59] op_sel_hi:[1,0]\n"
        "v_pk_mul_f32 v[140:141], v[140:141], v[58:59] op_sel_hi:[1,0]\n"
        "v_pk_mul_f32 v[142:143], v[142:143], v[58:59] op_sel_hi:[1,0]\n"
        "v_pk_mul_f32 v[144:145], v[144:145], v[58:59] op_sel_hi:[1,0]\n"
        "v_cvt_pk_bf16_f32 v60, v136, v137\n"
        "v_cvt_pk_bf16_f32 v61, v138, v139\n"
        "v_cvt_pk_bf16_f32 v62, v140, v141\n"
        "v_cvt_pk_bf16_f32 v63, v142, v143\n"
        "v_cvt_pk_bf16_f32 v59, v144, v145\n"
        "v_mov_b32 v237, 28\n"
        "v_exp_f32 v90, v90\n"
        "v_exp_f32 v91, v91\n"
        "v_cvt_pk_bf16_f32 v132, v88, v89\n"
        "v_exp_f32 v92, v92\n"
        "v_exp_f32 v93, v93\n"
        "v_mfma_f32_32x32x16_bf16 v[152:167], v[124:127], v[128:131], v[152:167]\n"
        "ds_read_b128 v[124:127], v248 offset:10240\n"
        "s_cbranch_vccnz L_SLOW0\n"
        "L_SBACK0:\n"
        "buffer_store_dwordx4 v[60:63], v238, s[28:31], s68 offen sc1\n"
        "buffer_store_dword v59, v243, s[28:31], s72 offen sc1\n"
        "ds_write_b32 v251, v237\n"
        "v_exp_f32 v94, v94\n"
        "v_cvt_pk_bf16_f32 v133, v90, v91\n"
        "v_exp_f32 v95, v95\n"
        "v_cvt_pk_bf16_f32 v134, v92, v93\n"
        "v_cvt_pk_bf16_f32 v135, v94, v95\n"
        "v_exp_f32 v64, v64\n"
        "v_exp_f32 v65, v65\n"
        "s_waitcnt lgkmcnt(2)\n"
        "v_mfma_f32_32x32x16_bf16 v[152:167], v[168:171], v[132:135], v[152:167]\n"
        "ds_read_b128 v[168:171], v248 offset:12800\n"
        "v_exp_f32 v66, v66\n"
        "v_exp_f32 v67, v67\n"
        "v_cvt_pk_bf16_f32 v128, v64, v65\n"
        "v_exp_f32 v68, v68\n"
        "v_exp_f32 v69, v69\n"
        "v_mfma_f32_32x32x16_bf16 v[80:95], v[116:119], v[212:215], 0\n"
        "ds_read_b128 v[116:119], v238 offset:32768\n"
        "v_exp_f32 v70, v70\n"
        "v_cvt_pk_bf16_f32 v129, v66, v67\n"
        "v_exp_f32 v71, v71\n"
        "v_cvt_pk_bf16_f32 v130, v68, v69\n"
        "v_exp_f32 v72, v72\n"
        "v_cvt_pk_bf16_f32 v131, v70, v71\n"
        "v_exp_f32 v73, v73\n"
        "v_mfma_f32_32x32x8_bf16 v[80:95], v[106:107], v[50:51], v[80:95]\n"
        "v_exp_f32 v74, v74\n"
        "v_exp_f32 v75, v75\n"
        "v_cvt_pk_bf16_f32 v132, v72, v73\n"
        "v_exp_f32 v76, v76\n"
        "v_exp_f32 v77, v77\n"
        "v_mfma_f32_32x32x16_bf16 v[152:167], v[120:123], v[128:131], v[152:167]\n"
        "ds_read_b128 v[120:123], v248 offset:8192\n"
        "v_exp_f32 v78, v78\n"
        "v_cvt_pk_bf16_f32 v133, v74, v75\n"
        "v_exp_f32 v79, v79\n"
        "v_cvt_pk_bf16_f32 v134, v76, v77\n"
        "v_cvt_pk_bf16_f32 v135, v78, v79\n"
        "v_exp_f32 v80, v80\n"
        "v_exp_f32 v81, v81\n"
        "s_waitcnt lgkmcnt(1)\n"
        "v_mfma_f32_32x32x16_bf16 v[152:167], v[168:171], v[132:135], v[152:167]\n"
        "ds_read_b128 v[168:171], v248 offset:10752\n"
        "v_exp_f32 v82, v82\n"
        "v_exp_f32 v83, v83\n"
        "v_cvt_pk_bf16_f32 v128, v80, v81\n"
        "v_exp_f32 v84, v84\n"
        "v_exp_f32 v85, v85\n"
        "v_mfma_f32_32x32x16_bf16 v[64:79], v[116:119], v[212:215], 0\n"
        "ds_read_b128 v[116:119], v238 offset:30720\n"
        "v_exp_f32 v86, v86\n"
        "v_cvt_pk_bf16_f32 v129, v82, v83\n"
        "v_exp_f32 v87, v87\n"
        "v_cvt_pk_bf16_f32 v130, v84, v85\n"
        "v_exp_f32 v88, v88\n"
        "v_cvt_pk_bf16_f32 v131, v86, v87\n"
        "v_exp_f32 v89, v89\n"
        "v_mfma_f32_32x32x8_bf16 v[64:79], v[104:105], v[50:51], v[64:79]\n"
        "v_exp_f32 v90, v90\n"
        "v_exp_f32 v91, v91\n"
        "v_cvt_pk_bf16_f32 v132, v88, v89\n"
        "v_exp_f32 v92, v92\n"
        "v_exp_f32 v93, v93\n"
        "v_mfma_f32_32x32x16_bf16 v[152:167], v[124:127], v[128:131], v[152:167]\n"
        "ds_read_b128 v[124:127], v248 offset:6144\n"
        "v_exp_f32 v94, v94\n"
        "v_cvt_pk_bf16_f32 v133, v90, v91\n"
        "v_exp_f32 v95, v95\n"
        "v_cvt_pk_bf16_f32 v134, v92, v93\n"
        "v_cvt_pk_bf16_f32 v135, v94, v95\n"
        "v_exp_f32 v64, v64\n"
        "v_exp_f32 v65, v65\n"
        "s_waitcnt lgkmcnt(1)\n"
        "v_mfma_f32_32x32x16_bf16 v[152:167], v[168:171], v[132:135], v[152:167]\n"
        "ds_read_b128 v[168:171], v248 offset:8704\n"
        "v_exp_f32 v66, v66\n"
        "v_exp_f32 v67, v67\n"
        "v_cvt_pk_bf16_f32 v128, v64, v65\n"
        "v_exp_f32 v68, v68\n"
        "v_exp_f32 v69, v69\n"
        "v_mfma_f32_32x32x16_bf16 v[80:95], v[116:119], v[212:215], 0\n"
        "ds_read_b128 v[116:119], v238 offset:28672\n"
        "v_exp_f32 v70, v70\n"
        "v_cvt_pk_bf16_f32 v129, v66, v67\n"
        "v_exp_f32 v71, v71\n"
        "v_cvt_pk_bf16_f32 v130, v68, v69\n"
        "v_exp_f32 v72, v72\n"
        "v_cvt_pk_bf16_f32 v131, v70, v71\n"
        "v_exp_f32 v73, v73\n"
        "v_mfma_f32_32x32x8_bf16 v[80:95], v[102:103], v[50:51], v[80:95]\n"
        "s_waitcnt vmcnt(0)\n"
        "buffer_store_dwordx2 v[244:245], v253, s[40:43], s55 offen sc1\n"
        "v_exp_f32 v74, v74\n"
        "v_exp_f32 v75, v75\n"
        "v_cvt_pk_bf16_f32 v132, v72, v73\n"
        "v_exp_f32 v76, v76\n"
        "v_exp_f32 v77, v77\n"
        "v_mfma_f32_32x32x16_bf16 v[152:167], v[120:123], v[128:131], v[152:167]\n"
        "ds_read_b128 v[120:123], v248 offset:4096\n"
        "v_exp_f32 v78, v78\n"
        "v_cvt_pk_bf16_f32 v133, v74, v75\n"
        "v_exp_f32 v79, v79\n"
        "v_cvt_pk_bf16_f32 v134, v76, v77\n"
        "v_cvt_pk_bf16_f32 v135, v78, v79\n"
        "v_exp_f32 v80, v80\n"
        "v_exp_f32 v81, v81\n"
        "s_waitcnt lgkmcnt(1)\n"
        "v_mfma_f32_32x32x16_bf16 v[152:167], v[168:171], v[132:135], v[152:167]\n"
        "ds_read_b128 v[168:171], v248 offset:6656\n"
        "v_exp_f32 v82, v82\n"
        "v_exp_f32 v83, v83\n"
        "v_cvt_pk_bf16_f32 v128, v80, v81\n"
        "v_exp_f32 v84, v84\n"
        "v_exp_f32 v85, v85\n"
        "v_mfma_f32_32x32x16_bf16 v[64:79], v[116:119], v[212:215], 0\n"
        "ds_read_b128 v[116:119], v238 offset:26624\n"
        "v_exp_f32 v86, v86\n"
        "v_cvt_pk_bf16_f32 v129, v82, v83\n"
        "v_exp_f32 v87, v87\n"
        "v_cvt_pk_bf16_f32 v130, v84, v85\n"
        "v_exp_f32 v88, v88\n"
        "v_cvt_pk_bf16_f32 v131, v86, v87\n"
        "v_exp_f32 v89, v89\n"
        "v_mfma_f32_32x32x8_bf16 v[64:79], v[100:101], v[50:51], v[64:79]\n"
        "v_exp_f32 v90, v90\n"
        "v_exp_f32 v91, v91\n"
        "v_cvt_pk_bf16_f32 v132, v88, v89\n"
        "v_exp_f32 v92, v92\n"
        "v_exp_f32 v93, v93\n"
        "v_mfma_f32_32x32x16_bf16 v[152:167], v[124:127], v[128:131], v[152:167]\n"
        "ds_read_b128 v[124:127], v248 offset:2048\n"
        "v_exp_f32 v94, v94\n"
        "v_cvt_pk_bf16_f32 v133, v90, v91\n"
        "v_exp_f32 v95, v95\n"
        "v_cvt_pk_bf16_f32 v134, v92, v93\n"
        "v_cvt_pk_bf16_f32 v135, v94, v95\n"
        "v_exp_f32 v64, v64\n"
        "v_exp_f32 v65, v65\n"
        "s_waitcnt lgkmcnt(1)\n"
        "v_mfma_f32_32x32x16_bf16 v[152:167], v[168:171], v[132:135], v[152:167]\n"
        "ds_read_b128 v[168:171], v248 offset:4608\n"
        "v_exp_f32 v66, v66\n"
        "v_exp_f32 v67, v67\n"
        "v_cvt_pk_bf16_f32 v128, v64, v65\n"
        "v_exp_f32 v68, v68\n"
        "v_exp_f32 v69, v69\n"
        "v_mfma_f32_32x32x16_bf16 v[80:95], v[116:119], v[212:215], 0\n"
        "ds_read_b128 v[116:119], v238 offset:24576\n"
        "v_exp_f32 v70, v70\n"
        "v_cvt_pk_bf16_f32 v129, v66, v67\n"
        "v_exp_f32 v71, v71\n"
        "v_cvt_pk_bf16_f32 v130, v68, v69\n"
        "v_exp_f32 v72, v72\n"
        "v_cvt_pk_bf16_f32 v131, v70, v71\n"
        "v_exp_f32 v73, v73\n"
        "v_mfma_f32_32x32x8_bf16 v[80:95], v[98:99], v[50:51], v[80:95]\n"
        "v_exp_f32 v74, v74\n"
        "v_exp_f32 v75, v75\n"
        "v_cvt_pk_bf16_f32 v132, v72, v73\n"
        "v_exp_f32 v76, v76\n"
        "v_exp_f32 v77, v77\n"
        "v_mfma_f32_32x32x16_bf16 v[152:167], v[120:123], v[128:131], v[152:167]\n"
        "ds_read_b128 v[120:123], v248 offset:0\n"
        "v_exp_f32 v78, v78\n"
        "v_cvt_pk_bf16_f32 v133, v74, v75\n"
        "v_exp_f32 v79, v79\n"
        "v_cvt_pk_bf16_f32 v134, v76, v77\n"
        "v_cvt_pk_bf16_f32 v135, v78, v79\n"
        "v_exp_f32 v80, v80\n"
        "v_exp_f32 v81, v81\n"
        "s_waitcnt lgkmcnt(1)\n"
        "v_mfma_f32_32x32x16_bf16 v[152:167], v[168:171], v[132:135], v[152:167]\n"
        "ds_read_b128 v[168:171], v248 offset:2560\n"
        "ds_read_b32 v236, v252\n"
        "v_exp_f32 v82, v82\n"
        "v_exp_f32 v83, v83\n"
        "v_cvt_pk_bf16_f32 v128, v80, v81\n"
        "v_exp_f32 v84, v84\n"
        "v_exp_f32 v85, v85\n"
        "v_mfma_f32_32x32x16_bf16 v[64:79], v[116:119], v[212:215], v[16:31]\n"
        "v_exp_f32 v86, v86\n"
        "v_cvt_pk_bf16_f32 v129, v82, v83\n"
        "v_exp_f32 v87, v87\n"
        "v_cvt_pk_bf16_f32 v130, v84, v85\n"
        "v_exp_f32 v88, v88\n"
        "v_cvt_pk_bf16_f32 v131, v86, v87\n"
        "v_exp_f32 v89, v89\n"
        "v_mfma_f32_32x32x8_bf16 v[64:79], v[96:97], v[50:51], v[64:79]\n"
        "v_exp_f32 v90, v90\n"
        "v_exp_f32 v91, v91\n"
        "v_cvt_pk_bf16_f32 v132, v88, v89\n"
        "v_exp_f32 v92, v92\n"
        "v_exp_f32 v93, v93\n"
        "s_waitcnt lgkmcnt(0)\n"
        "v_cmp_gt_u32 vcc, 9, v236\n"
        "v_mfma_f32_32x32x16_bf16 v[152:167], v[124:127], v[128:131], v[152:167]\n"
        "s_cbranch_vccnz Lslow_123\n"
        "Lback_124:\n"
        "ds_read_b128 v[116:119], v239 offset:14336\n"
        "ds_read_b128 v[124:127], v249 offset:14336\n"
        "v_exp_f32 v94, v94\n"
        "v_cvt_pk_bf16_f32 v133, v90, v91\n"
        "v_exp_f32 v95, v95\n"
        "v_cvt_pk_bf16_f32 v134, v92, v93\n"
        "v_cvt_pk_bf16_f32 v135, v94, v95\n"
        "v_exp_f32 v64, v64\n"
        "v_exp_f32 v65, v65\n"
        "v_mfma_f32_32x32x16_bf16 v[152:167], v[168:171], v[132:135], v[152:167]\n"
        "ds_read_b128 v[168:171], v248 offset:512\n"
        "v_exp_f32 v66, v66\n"
        "v_exp_f32 v67, v67\n"
        "v_cvt_pk_bf16_f32 v128, v64, v65\n"
        "v_exp_f32 v68, v68\n"
        "v_exp_f32 v69, v69\n"
        "s_waitcnt lgkmcnt(2)\n"
        "v_mfma_f32_32x32x16_bf16 v[80:95], v[116:119], v[216:219], 0\n"
        "ds_read_b128 v[116:119], v239 offset:12288\n"
        "v_exp_f32 v70, v70\n"
        "v_cvt_pk_bf16_f32 v129, v66, v67\n"
        "v_exp_f32 v71, v71\n"
        "v_cvt_pk_bf16_f32 v130, v68, v69\n"
        "v_exp_f32 v72, v72\n"
        "v_cvt_pk_bf16_f32 v131, v70, v71\n"
        "v_exp_f32 v73, v73\n"
        "v_mfma_f32_32x32x8_bf16 v[80:95], v[110:111], v[52:53], v[80:95]\n"
        "v_exp_f32 v74, v74\n"
        "v_exp_f32 v75, v75\n"
        "v_cvt_pk_bf16_f32 v132, v72, v73\n"
        "v_exp_f32 v76, v76\n"
        "v_exp_f32 v77, v77\n"
        "v_mfma_f32_32x32x16_bf16 v[152:167], v[120:123], v[128:131], v[152:167]\n"
        "ds_read_b128 v[120:123], v249 offset:12288\n"
        "v_exp_f32 v78, v78\n"
        "v_cvt_pk_bf16_f32 v133, v74, v75\n"
        "v_exp_f32 v79, v79\n"
        "v_cvt_pk_bf16_f32 v134, v76, v77\n"
        "v_cvt_pk_bf16_f32 v135, v78, v79\n"
        "v_exp_f32 v80, v80\n"
        "v_exp_f32 v81, v81\n"
        "s_waitcnt lgkmcnt(1)\n"
        "v_mfma_f32_32x32x16_bf16 v[152:167], v[168:171], v[132:135], v[152:167]\n"
        "ds_read_b128 v[168:171], v249 offset:14848\n"
        "v_exp_f32 v82, v82\n"
        "v_exp_f32 v83, v83\n"
        "v_cvt_pk_bf16_f32 v128, v80, v81\n"
        "v_exp_f32 v84, v84\n"
        "v_exp_f32 v85, v85\n"
        "v_mfma_f32_32x32x16_bf16 v[64:79], v[116:119], v[216:219], 0\n"
        "ds_read_b128 v[116:119], v239 offset:10240\n"
        "v_exp_f32 v86, v86\n"
        "v_cvt_pk_bf16_f32 v129, v82, v83\n"
        "v_exp_f32 v87, v87\n"
        "v_cvt_pk_bf16_f32 v130, v84, v85\n"
        "v_exp_f32 v88, v88\n"
        "v_cvt_pk_bf16_f32 v131, v86, v87\n"
        "v_exp_f32 v89, v89\n"
        "v_mfma_f32_32x32x8_bf16 v[64:79], v[108:109], v[52:53], v[64:79]\n"
        "v_rcp_f32 v58, v164\n"
        "v_maximum3_f32 v57, |v152|, |v153|, v164\n"
        "v_maximum3_f32 v57, |v154|, |v155|, v57\n"
        "v_maximum3_f32 v57, |v156|, |v157|, v57\n"
        "v_maximum3_f32 v57, |v158|, |v159|, v57\n"
        "v_maximum3_f32 v57, |v162|, |v163|, v57\n"
        "v_maximum3_f32 v57, v58, v57, v57\n"
        "v_cmp_ngt_f32 vcc, s62, v57\n"
        "v_pk_mul_f32 v[152:153], v[152:153], v[58:59] op_sel_hi:[1,0]\n"
        "v_pk_mul_f32 v[154:155], v[154:155], v[58:59] op_sel_hi:[1,0]\n"
        "v_pk_mul_f32 v[156:157], v[156:157], v[58:59] op_sel_hi:[1,0]\n"
        "v_pk_mul_f32 v[158:159], v[158:159], v[58:59] op_sel_hi:[1,0]\n"
        "v_pk_mul_f32 v[162:163], v[162:163], v[58:59] op_sel_hi:[1,0]\n"
        "v_cvt_pk_bf16_f32 v60, v152, v153\n"
        "v_cvt_pk_bf16_f32 v61, v154, v155\n"
        "v_cvt_pk_bf16_f32 v62, v156, v157\n"
        "v_cvt_pk_bf16_f32 v63, v158, v159\n"
        "v_cvt_pk_bf16_f32 v59, v162, v163\n"
        "v_mov_b32 v237, 29\n"
        "v_or_b32 v244, 2, v244\n"
        "v_exp_f32 v90, v90\n"
        "v_exp_f32 v91, v91\n"
        "v_cvt_pk_bf16_f32 v132, v88, v89\n"
        "v_exp_f32 v92, v92\n"
        "v_exp_f32 v93, v93\n"
        "v_mfma_f32_32x32x16_bf16 v[32:47], v[124:127], v[128:131], v[32:47]\n"
        "ds_read_b128 v[124:127], v249 offset:10240\n"
        "s_cbranch_vccnz L_SLOW1\n"
        "L_SBACK1:\n"
        "buffer_store_dwordx4 v[60:63], v238, s[28:31], s69 offen sc1\n"
        "buffer_store_dword v59, v243, s[28:31], s73 offen sc1\n"
        "ds_write_b32 v251, v237\n"
        "v_exp_f32 v94, v94\n"
        "v_cvt_pk_bf16_f32 v133, v90, v91\n"
        "v_exp_f32 v95, v95\n"
        "v_cvt_pk_bf16_f32 v134, v92, v93\n"
        "v_cvt_pk_bf16_f32 v135, v94, v95\n"
        "v_exp_f32 v64, v64\n"
        "v_exp_f32 v65, v65\n"
        "s_waitcnt lgkmcnt(2)\n"
        "v_mfma_f32_32x32x16_bf16 v[32:47], v[168:171], v[132:135], v[32:47]\n"
        "ds_read_b128 v[168:171], v249 offset:12800\n"
        "v_exp_f32 v66, v66\n"
        "v_exp_f32 v67, v67\n"
        "v_cvt_pk_bf16_f32 v128, v64, v65\n"
        "v_exp_f32 v68, v68\n"
        "v_exp_f32 v69, v69\n"
        "v_mfma_f32_32x32x16_bf16 v[80:95], v[116:119], v[216:219], 0\n"
        "ds_read_b128 v[116:119], v239 offset:8192\n"
        "v_exp_f32 v70, v70\n"
        "v_cvt_pk_bf16_f32 v129, v66, v67\n"
        "v_exp_f32 v71, v71\n"
        "v_cvt_pk_bf16_f32 v130, v68, v69\n"
        "v_exp_f32 v72, v72\n"
        "v_cvt_pk_bf16_f32 v131, v70, v71\n"
        "v_exp_f32 v73, v73\n"
        "v_mfma_f32_32x32x8_bf16 v[80:95], v[106:107], v[52:53], v[80:95]\n"
        "v_exp_f32 v74, v74\n"
        "v_exp_f32 v75, v75\n"
        "v_cvt_pk_bf16_f32 v132, v72, v73\n"
        "v_exp_f32 v76, v76\n"
        "v_exp_f32 v77, v77\n"
        "v_mfma_f32_32x32x16_bf16 v[32:47], v[120:123], v[128:131], v[32:47]\n"
        "ds_read_b128 v[120:123], v249 offset:8192\n"
        "v_exp_f32 v78, v78\n"
        "v_cvt_pk_bf16_f32 v133, v74, v75\n"
        "v_exp_f32 v79, v79\n"
        "v_cvt_pk_bf16_f32 v134, v76, v77\n"
        "v_cvt_pk_bf16_f32 v135, v78, v79\n"
        "v_exp_f32 v80, v80\n"
        "v_exp_f32 v81, v81\n"
        "s_waitcnt lgkmcnt(1)\n"
        "v_mfma_f32_32x32x16_bf16 v[32:47], v[168:171], v[132:135], v[32:47]\n"
        "ds_read_b128 v[168:171], v249 offset:10752\n"
        "v_exp_f32 v82, v82\n"
        "v_exp_f32 v83, v83\n"
        "v_cvt_pk_bf16_f32 v128, v80, v81\n"
        "v_exp_f32 v84, v84\n"
        "v_exp_f32 v85, v85\n"
        "v_mfma_f32_32x32x16_bf16 v[64:79], v[116:119], v[216:219], 0\n"
        "ds_read_b128 v[116:119], v239 offset:6144\n"
        "v_exp_f32 v86, v86\n"
        "v_cvt_pk_bf16_f32 v129, v82, v83\n"
        "v_exp_f32 v87, v87\n"
        "v_cvt_pk_bf16_f32 v130, v84, v85\n"
        "v_exp_f32 v88, v88\n"
        "v_cvt_pk_bf16_f32 v131, v86, v87\n"
        "v_exp_f32 v89, v89\n"
        "v_mfma_f32_32x32x8_bf16 v[64:79], v[104:105], v[52:53], v[64:79]\n"
        "v_exp_f32 v90, v90\n"
        "v_exp_f32 v91, v91\n"
        "v_cvt_pk_bf16_f32 v132, v88, v89\n"
        "v_exp_f32 v92, v92\n"
        "v_exp_f32 v93, v93\n"
        "v_mfma_f32_32x32x16_bf16 v[32:47], v[124:127], v[128:131], v[32:47]\n"
        "ds_read_b128 v[124:127], v249 offset:6144\n"
        "v_exp_f32 v94, v94\n"
        "v_cvt_pk_bf16_f32 v133, v90, v91\n"
        "v_exp_f32 v95, v95\n"
        "v_cvt_pk_bf16_f32 v134, v92, v93\n"
        "v_cvt_pk_bf16_f32 v135, v94, v95\n"
        "v_exp_f32 v64, v64\n"
        "v_exp_f32 v65, v65\n"
        "s_waitcnt lgkmcnt(1)\n"
        "v_mfma_f32_32x32x16_bf16 v[32:47], v[168:171], v[132:135], v[32:47]\n"
        "ds_read_b128 v[168:171], v249 offset:8704\n"
        "v_exp_f32 v66, v66\n"
        "v_exp_f32 v67, v67\n"
        "v_cvt_pk_bf16_f32 v128, v64, v65\n"
        "v_exp_f32 v68, v68\n"
        "v_exp_f32 v69, v69\n"
        "v_mfma_f32_32x32x16_bf16 v[80:95], v[116:119], v[216:219], 0\n"
        "ds_read_b128 v[116:119], v239 offset:4096\n"
        "v_exp_f32 v70, v70\n"
        "v_cvt_pk_bf16_f32 v129, v66, v67\n"
        "v_exp_f32 v71, v71\n"
        "v_cvt_pk_bf16_f32 v130, v68, v69\n"
        "v_exp_f32 v72, v72\n"
        "v_cvt_pk_bf16_f32 v131, v70, v71\n"
        "v_exp_f32 v73, v73\n"
        "v_mfma_f32_32x32x8_bf16 v[80:95], v[102:103], v[52:53], v[80:95]\n"
        "s_waitcnt vmcnt(0)\n"
        "buffer_store_dwordx2 v[244:245], v253, s[40:43], s55 offen sc1\n"
        "v_exp_f32 v74, v74\n"
        "v_exp_f32 v75, v75\n"
        "v_cvt_pk_bf16_f32 v132, v72, v73\n"
        "v_exp_f32 v76, v76\n"
        "v_exp_f32 v77, v77\n"
        "v_mfma_f32_32x32x16_bf16 v[32:47], v[120:123], v[128:131], v[32:47]\n"
        "ds_read_b128 v[120:123], v249 offset:4096\n"
        "v_exp_f32 v78, v78\n"
        "v_cvt_pk_bf16_f32 v133, v74, v75\n"
        "v_exp_f32 v79, v79\n"
        "v_cvt_pk_bf16_f32 v134, v76, v77\n"
        "v_cvt_pk_bf16_f32 v135, v78, v79\n"
        "v_exp_f32 v80, v80\n"
        "v_exp_f32 v81, v81\n"
        "s_waitcnt lgkmcnt(1)\n"
        "v_mfma_f32_32x32x16_bf16 v[32:47], v[168:171], v[132:135], v[32:47]\n"
        "ds_read_b128 v[168:171], v249 offset:6656\n"
        "v_exp_f32 v82, v82\n"
        "v_exp_f32 v83, v83\n"
        "v_cvt_pk_bf16_f32 v128, v80, v81\n"
        "v_exp_f32 v84, v84\n"
        "v_exp_f32 v85, v85\n"
        "v_mfma_f32_32x32x16_bf16 v[64:79], v[116:119], v[216:219], 0\n"
        "ds_read_b128 v[116:119], v239 offset:2048\n"
        "v_exp_f32 v86, v86\n"
        "v_cvt_pk_bf16_f32 v129, v82, v83\n"
        "v_exp_f32 v87, v87\n"
        "v_cvt_pk_bf16_f32 v130, v84, v85\n"
        "v_exp_f32 v88, v88\n"
        "v_cvt_pk_bf16_f32 v131, v86, v87\n"
        "v_exp_f32 v89, v89\n"
        "v_mfma_f32_32x32x8_bf16 v[64:79], v[100:101], v[52:53], v[64:79]\n"
        "v_exp_f32 v90, v90\n"
        "v_exp_f32 v91, v91\n"
        "v_cvt_pk_bf16_f32 v132, v88, v89\n"
        "v_exp_f32 v92, v92\n"
        "v_exp_f32 v93, v93\n"
        "v_mfma_f32_32x32x16_bf16 v[32:47], v[124:127], v[128:131], v[32:47]\n"
        "ds_read_b128 v[124:127], v249 offset:2048\n"
        "v_exp_f32 v94, v94\n"
        "v_cvt_pk_bf16_f32 v133, v90, v91\n"
        "v_exp_f32 v95, v95\n"
        "v_cvt_pk_bf16_f32 v134, v92, v93\n"
        "v_cvt_pk_bf16_f32 v135, v94, v95\n"
        "v_exp_f32 v64, v64\n"
        "v_exp_f32 v65, v65\n"
        "s_waitcnt lgkmcnt(1)\n"
        "v_mfma_f32_32x32x16_bf16 v[32:47], v[168:171], v[132:135], v[32:47]\n"
        "ds_read_b128 v[168:171], v249 offset:4608\n"
        "v_exp_f32 v66, v66\n"
        "v_exp_f32 v67, v67\n"
        "v_cvt_pk_bf16_f32 v128, v64, v65\n"
        "v_exp_f32 v68, v68\n"
        "v_exp_f32 v69, v69\n"
        "v_mfma_f32_32x32x16_bf16 v[80:95], v[116:119], v[216:219], 0\n"
        "ds_read_b128 v[116:119], v239 offset:0\n"
        "v_exp_f32 v70, v70\n"
        "v_cvt_pk_bf16_f32 v129, v66, v67\n"
        "v_exp_f32 v71, v71\n"
        "v_cvt_pk_bf16_f32 v130, v68, v69\n"
        "v_exp_f32 v72, v72\n"
        "v_cvt_pk_bf16_f32 v131, v70, v71\n"
        "v_exp_f32 v73, v73\n"
        "v_mfma_f32_32x32x8_bf16 v[80:95], v[98:99], v[52:53], v[80:95]\n"
        "v_exp_f32 v74, v74\n"
        "v_exp_f32 v75, v75\n"
        "v_cvt_pk_bf16_f32 v132, v72, v73\n"
        "v_exp_f32 v76, v76\n"
        "v_exp_f32 v77, v77\n"
        "v_mfma_f32_32x32x16_bf16 v[32:47], v[120:123], v[128:131], v[32:47]\n"
        "ds_read_b128 v[120:123], v249 offset:0\n"
        "v_exp_f32 v78, v78\n"
        "v_cvt_pk_bf16_f32 v133, v74, v75\n"
        "v_exp_f32 v79, v79\n"
        "v_cvt_pk_bf16_f32 v134, v76, v77\n"
        "v_cvt_pk_bf16_f32 v135, v78, v79\n"
        "v_exp_f32 v80, v80\n"
        "v_exp_f32 v81, v81\n"
        "s_waitcnt lgkmcnt(1)\n"
        "v_mfma_f32_32x32x16_bf16 v[32:47], v[168:171], v[132:135], v[32:47]\n"
        "ds_read_b128 v[168:171], v249 offset:2560\n"
        "v_exp_f32 v82, v82\n"
        "v_exp_f32 v83, v83\n"
        "v_cvt_pk_bf16_f32 v128, v80, v81\n"
        "v_exp_f32 v84, v84\n"
        "v_exp_f32 v85, v85\n"
        "v_mfma_f32_32x32x16_bf16 v[64:79], v[116:119], v[216:219], v[16:31]\n"
        "ds_read_b128 v[116:119], v239 offset:40960\n"
        "v_exp_f32 v86, v86\n"
        "v_cvt_pk_bf16_f32 v129, v82, v83\n"
        "v_exp_f32 v87, v87\n"
        "v_cvt_pk_bf16_f32 v130, v84, v85\n"
        "v_exp_f32 v88, v88\n"
        "v_cvt_pk_bf16_f32 v131, v86, v87\n"
        "v_exp_f32 v89, v89\n"
        "v_mfma_f32_32x32x8_bf16 v[64:79], v[96:97], v[52:53], v[64:79]\n"
        "v_exp_f32 v90, v90\n"
        "v_exp_f32 v91, v91\n"
        "v_cvt_pk_bf16_f32 v132, v88, v89\n"
        "v_exp_f32 v92, v92\n"
        "v_exp_f32 v93, v93\n"
        "v_mfma_f32_32x32x16_bf16 v[32:47], v[124:127], v[128:131], v[32:47]\n"
        "ds_read_b128 v[124:127], v250 offset:16384\n"
        "v_exp_f32 v94, v94\n"
        "v_cvt_pk_bf16_f32 v133, v90, v91\n"
        "v_exp_f32 v95, v95\n"
        "v_cvt_pk_bf16_f32 v134, v92, v93\n"
        "v_cvt_pk_bf16_f32 v135, v94, v95\n"
        "v_exp_f32 v64, v64\n"
        "v_exp_f32 v65, v65\n"
        "s_waitcnt lgkmcnt(1)\n"
        "v_mfma_f32_32x32x16_bf16 v[32:47], v[168:171], v[132:135], v[32:47]\n"
        "ds_read_b128 v[168:171], v249 offset:512\n"
        "ds_read_b32 v236, v252\n"
        "v_exp_f32 v66, v66\n"
        "v_exp_f32 v67, v67\n"
        "v_cvt_pk_bf16_f32 v128, v64, v65\n"
        "v_exp_f32 v68, v68\n"
        "v_exp_f32 v69, v69\n"
        "v_mfma_f32_32x32x16_bf16 v[80:95], v[116:119], v[220:223], v[0:15]\n"
        "v_exp_f32 v70, v70\n"
        "v_cvt_pk_bf16_f32 v129, v66, v67\n"
        "v_exp_f32 v71, v71\n"
        "v_cvt_pk_bf16_f32 v130, v68, v69\n"
        "v_exp_f32 v72, v72\n"
        "v_cvt_pk_bf16_f32 v131, v70, v71\n"
        "v_exp_f32 v73, v73\n"
        "v_mfma_f32_32x32x8_bf16 v[80:95], v[112:113], v[54:55], v[80:95]\n"
        "v_exp_f32 v74, v74\n"
        "v_exp_f32 v75, v75\n"
        "v_cvt_pk_bf16_f32 v132, v72, v73\n"
        "v_exp_f32 v76, v76\n"
        "v_exp_f32 v77, v77\n"
        "s_waitcnt lgkmcnt(0)\n"
        "v_cmp_gt_u32 vcc, 10, v236\n"
        "v_mfma_f32_32x32x16_bf16 v[32:47], v[120:123], v[128:131], v[32:47]\n"
        "s_cbranch_vccnz Lslow_125\n"
        "Lback_126:\n"
        "ds_read_b128 v[116:119], v239 offset:38912\n"
        "ds_read_b128 v[120:123], v250 offset:14336\n"
        "v_exp_f32 v78, v78\n"
        "v_cvt_pk_bf16_f32 v133, v74, v75\n"
        "v_exp_f32 v79, v79\n"
        "v_cvt_pk_bf16_f32 v134, v76, v77\n"
        "v_cvt_pk_bf16_f32 v135, v78, v79\n"
        "v_exp_f32 v80, v80\n"
        "v_exp_f32 v81, v81\n"
        "v_mfma_f32_32x32x16_bf16 v[32:47], v[168:171], v[132:135], v[32:47]\n"
        "ds_read_b128 v[168:171], v250 offset:16896\n"
        "v_exp_f32 v82, v82\n"
        "v_exp_f32 v83, v83\n"
        "v_cvt_pk_bf16_f32 v128, v80, v81\n"
        "v_exp_f32 v84, v84\n"
        "v_exp_f32 v85, v85\n"
        "s_waitcnt lgkmcnt(2)\n"
        "v_mfma_f32_32x32x16_bf16 v[64:79], v[116:119], v[220:223], 0\n"
        "ds_read_b128 v[116:119], v239 offset:36864\n"
        "v_exp_f32 v86, v86\n"
        "v_cvt_pk_bf16_f32 v129, v82, v83\n"
        "v_exp_f32 v87, v87\n"
        "v_cvt_pk_bf16_f32 v130, v84, v85\n"
        "v_exp_f32 v88, v88\n"
        "v_cvt_pk_bf16_f32 v131, v86, v87\n"
        "v_exp_f32 v89, v89\n"
        "v_mfma_f32_32x32x8_bf16 v[64:79], v[110:111], v[54:55], v[64:79]\n"
        "v_rcp_f32 v58, v44\n"
        "v_maximum3_f32 v57, |v32|, |v33|, v44\n"
        "v_maximum3_f32 v57, |v34|, |v35|, v57\n"
        "v_maximum3_f32 v57, |v36|, |v37|, v57\n"
        "v_maximum3_f32 v57, |v38|, |v39|, v57\n"
        "v_maximum3_f32 v57, |v40|, |v41|, v57\n"
        "v_maximum3_f32 v57, v58, v57, v57\n"
        "v_cmp_ngt_f32 vcc, s62, v57\n"
        "v_pk_mul_f32 v[32:33], v[32:33], v[58:59] op_sel_hi:[1,0]\n"
        "v_pk_mul_f32 v[34:35], v[34:35], v[58:59] op_sel_hi:[1,0]\n"
        "v_pk_mul_f32 v[36:37], v[36:37], v[58:59] op_sel_hi:[1,0]\n"
        "v_pk_mul_f32 v[38:39], v[38:39], v[58:59] op_sel_hi:[1,0]\n"
        "v_pk_mul_f32 v[40:41], v[40:41], v[58:59] op_sel_hi:[1,0]\n"
        "v_cvt_pk_bf16_f32 v60, v32, v33\n"
        "v_cvt_pk_bf16_f32 v61, v34, v35\n"
        "v_cvt_pk_bf16_f32 v62, v36, v37\n"
        "v_cvt_pk_bf16_f32 v63, v38, v39\n"
        "v_cvt_pk_bf16_f32 v59, v40, v41\n"
        "v_mov_b32 v237, 30\n"
        "v_or_b32 v244, 4, v244\n"
        "v_xor_b32 v243, 0x10000000, v243\n"
        "v_exp_f32 v90, v90\n"
        "v_exp_f32 v91, v91\n"
        "v_cvt_pk_bf16_f32 v132, v88, v89\n"
        "v_exp_f32 v92, v92\n"
        "v_exp_f32 v93, v93\n"
        "v_mfma_f32_32x32x16_bf16 v[192:207], v[124:127], v[128:131], 0\n"
        "ds_read_b128 v[124:127], v250 offset:12288\n"
        "s_cbranch_vccnz L_SLOW2\n"
        "L_SBACK2:\n"
        "buffer_store_dwordx4 v[60:63], v238, s[28:31], s70 offen sc1\n"
        "buffer_store_dword v59, v243, s[28:31], s74 offen sc1\n"
        "ds_write_b32 v251, v237\n"
        "v_exp_f32 v94, v94\n"
        "v_cvt_pk_bf16_f32 v133, v90, v91\n"
        "v_exp_f32 v95, v95\n"
        "v_cvt_pk_bf16_f32 v134, v92, v93\n"
        "v_cvt_pk_bf16_f32 v135, v94, v95\n"
        "v_exp_f32 v64, v64\n"
        "v_exp_f32 v65, v65\n"
        "s_waitcnt lgkmcnt(2)\n"
        "v_mfma_f32_32x32x16_bf16 v[192:207], v[168:171], v[132:135], v[192:207]\n"
        "ds_read_b128 v[168:171], v250 offset:14848\n"
        "v_exp_f32 v66, v66\n"
        "v_exp_f32 v67, v67\n"
        "v_cvt_pk_bf16_f32 v128, v64, v65\n"
        "v_exp_f32 v68, v68\n"
        "v_exp_f32 v69, v69\n"
        "v_mfma_f32_32x32x16_bf16 v[80:95], v[116:119], v[220:223], 0\n"
        "ds_read_b128 v[116:119], v239 offset:34816\n"
        "v_exp_f32 v70, v70\n"
        "v_cvt_pk_bf16_f32 v129, v66, v67\n"
        "v_exp_f32 v71, v71\n"
        "v_cvt_pk_bf16_f32 v130, v68, v69\n"
        "v_exp_f32 v72, v72\n"
        "v_cvt_pk_bf16_f32 v131, v70, v71\n"
        "v_exp_f32 v73, v73\n"
        "v_mfma_f32_32x32x8_bf16 v[80:95], v[108:109], v[54:55], v[80:95]\n"
        "v_exp_f32 v74, v74\n"
        "v_exp_f32 v75, v75\n"
        "v_cvt_pk_bf16_f32 v132, v72, v73\n"
        "v_exp_f32 v76, v76\n"
        "v_exp_f32 v77, v77\n"
        "v_mfma_f32_32x32x16_bf16 v[192:207], v[120:123], v[128:131], v[192:207]\n"
        "ds_read_b128 v[120:123], v250 offset:10240\n"
        "v_exp_f32 v78, v78\n"
        "v_cvt_pk_bf16_f32 v133, v74, v75\n"
        "v_exp_f32 v79, v79\n"
        "v_cvt_pk_bf16_f32 v134, v76, v77\n"
        "v_cvt_pk_bf16_f32 v135, v78, v79\n"
        "v_exp_f32 v80, v80\n"
        "v_exp_f32 v81, v81\n"
        "s_waitcnt lgkmcnt(1)\n"
        "v_mfma_f32_32x32x16_bf16 v[192:207], v[168:171], v[132:135], v[192:207]\n"
        "ds_read_b128 v[168:171], v250 offset:12800\n"
        "v_exp_f32 v82, v82\n"
        "v_exp_f32 v83, v83\n"
        "v_cvt_pk_bf16_f32 v128, v80, v81\n"
        "v_exp_f32 v84, v84\n"
        "v_exp_f32 v85, v85\n"
        "v_mfma_f32_32x32x16_bf16 v[64:79], v[116:119], v[220:223], 0\n"
        "ds_read_b128 v[116:119], v239 offset:32768\n"
        "v_exp_f32 v86, v86\n"
        "v_cvt_pk_bf16_f32 v129, v82, v83\n"
        "v_exp_f32 v87, v87\n"
        "v_cvt_pk_bf16_f32 v130, v84, v85\n"
        "v_exp_f32 v88, v88\n"
        "v_cvt_pk_bf16_f32 v131, v86, v87\n"
        "v_exp_f32 v89, v89\n"
        "v_mfma_f32_32x32x8_bf16 v[64:79], v[106:107], v[54:55], v[64:79]\n"
        "v_exp_f32 v90, v90\n"
        "v_exp_f32 v91, v91\n"
        "v_cvt_pk_bf16_f32 v132, v88, v89\n"
        "v_exp_f32 v92, v92\n"
        "v_exp_f32 v93, v93\n"
        "v_mfma_f32_32x32x16_bf16 v[192:207], v[124:127], v[128:131], v[192:207]\n"
        "ds_read_b128 v[124:127], v250 offset:8192\n"
        "v_exp_f32 v94, v94\n"
        "v_cvt_pk_bf16_f32 v133, v90, v91\n"
        "v_exp_f32 v95, v95\n"
        "v_cvt_pk_bf16_f32 v134, v92, v93\n"
        "v_cvt_pk_bf16_f32 v135, v94, v95\n"
        "v_exp_f32 v64, v64\n"
        "v_exp_f32 v65, v65\n"
        "s_waitcnt lgkmcnt(1)\n"
        "v_mfma_f32_32x32x16_bf16 v[192:207], v[168:171], v[132:135], v[192:207]\n"
        "ds_read_b128 v[168:171], v250 offset:10752\n"
        "v_exp_f32 v66, v66\n"
        "v_exp_f32 v67, v67\n"
        "v_cvt_pk_bf16_f32 v128, v64, v65\n"
        "v_exp_f32 v68, v68\n"
        "v_exp_f32 v69, v69\n"
        "v_mfma_f32_32x32x16_bf16 v[80:95], v[116:119], v[220:223], 0\n"
        "ds_read_b128 v[116:119], v239 offset:30720\n"
        "v_exp_f32 v70, v70\n"
        "v_cvt_pk_bf16_f32 v129, v66, v67\n"
        "v_exp_f32 v71, v71\n"
        "v_cvt_pk_bf16_f32 v130, v68, v69\n"
        "v_exp_f32 v72, v72\n"
        "v_cvt_pk_bf16_f32 v131, v70, v71\n"
        "v_exp_f32 v73, v73\n"
        "v_mfma_f32_32x32x8_bf16 v[80:95], v[104:105], v[54:55], v[80:95]\n"
        "s_waitcnt vmcnt(0)\n"
        "buffer_store_dwordx2 v[244:245], v253, s[40:43], s55 offen sc1\n"
        "v_exp_f32 v74, v74\n"
        "v_exp_f32 v75, v75\n"
        "v_cvt_pk_bf16_f32 v132, v72, v73\n"
        "v_exp_f32 v76, v76\n"
        "v_exp_f32 v77, v77\n"
        "v_mfma_f32_32x32x16_bf16 v[192:207], v[120:123], v[128:131], v[192:207]\n"
        "ds_read_b128 v[120:123], v250 offset:6144\n"
        "v_exp_f32 v78, v78\n"
        "v_cvt_pk_bf16_f32 v133, v74, v75\n"
        "v_exp_f32 v79, v79\n"
        "v_cvt_pk_bf16_f32 v134, v76, v77\n"
        "v_cvt_pk_bf16_f32 v135, v78, v79\n"
        "v_exp_f32 v80, v80\n"
        "v_exp_f32 v81, v81\n"
        "s_waitcnt lgkmcnt(1)\n"
        "v_mfma_f32_32x32x16_bf16 v[192:207], v[168:171], v[132:135], v[192:207]\n"
        "ds_read_b128 v[168:171], v250 offset:8704\n"
        "v_exp_f32 v82, v82\n"
        "v_exp_f32 v83, v83\n"
        "v_cvt_pk_bf16_f32 v128, v80, v81\n"
        "v_exp_f32 v84, v84\n"
        "v_exp_f32 v85, v85\n"
        "v_mfma_f32_32x32x16_bf16 v[64:79], v[116:119], v[220:223], 0\n"
        "ds_read_b128 v[116:119], v239 offset:28672\n"
        "v_exp_f32 v86, v86\n"
        "v_cvt_pk_bf16_f32 v129, v82, v83\n"
        "v_exp_f32 v87, v87\n"
        "v_cvt_pk_bf16_f32 v130, v84, v85\n"
        "v_exp_f32 v88, v88\n"
        "v_cvt_pk_bf16_f32 v131, v86, v87\n"
        "v_exp_f32 v89, v89\n"
        "v_mfma_f32_32x32x8_bf16 v[64:79], v[102:103], v[54:55], v[64:79]\n"
        "v_exp_f32 v90, v90\n"
        "v_exp_f32 v91, v91\n"
        "v_cvt_pk_bf16_f32 v132, v88, v89\n"
        "v_exp_f32 v92, v92\n"
        "v_exp_f32 v93, v93\n"
        "v_mfma_f32_32x32x16_bf16 v[192:207], v[124:127], v[128:131], v[192:207]\n"
        "ds_read_b128 v[124:127], v250 offset:4096\n"
        "v_exp_f32 v94, v94\n"
        "v_cvt_pk_bf16_f32 v133, v90, v91\n"
        "v_exp_f32 v95, v95\n"
        "v_cvt_pk_bf16_f32 v134, v92, v93\n"
        "v_cvt_pk_bf16_f32 v135, v94, v95\n"
        "v_exp_f32 v64, v64\n"
        "v_exp_f32 v65, v65\n"
        "s_waitcnt lgkmcnt(1)\n"
        "v_mfma_f32_32x32x16_bf16 v[192:207], v[168:171], v[132:135], v[192:207]\n"
        "ds_read_b128 v[168:171], v250 offset:6656\n"
        "v_exp_f32 v66, v66\n"
        "v_exp_f32 v67, v67\n"
        "v_cvt_pk_bf16_f32 v128, v64, v65\n"
        "v_exp_f32 v68, v68\n"
        "v_exp_f32 v69, v69\n"
        "v_mfma_f32_32x32x16_bf16 v[80:95], v[116:119], v[220:223], 0\n"
        "ds_read_b128 v[116:119], v239 offset:26624\n"
        "v_exp_f32 v70, v70\n"
        "v_cvt_pk_bf16_f32 v129, v66, v67\n"
        "v_exp_f32 v71, v71\n"
        "v_cvt_pk_bf16_f32 v130, v68, v69\n"
        "v_exp_f32 v72, v72\n"
        "v_cvt_pk_bf16_f32 v131, v70, v71\n"
        "v_exp_f32 v73, v73\n"
        "v_mfma_f32_32x32x8_bf16 v[80:95], v[100:101], v[54:55], v[80:95]\n"
        "v_exp_f32 v74, v74\n"
        "v_exp_f32 v75, v75\n"
        "v_cvt_pk_bf16_f32 v132, v72, v73\n"
        "v_exp_f32 v76, v76\n"
        "v_exp_f32 v77, v77\n"
        "v_mfma_f32_32x32x16_bf16 v[192:207], v[120:123], v[128:131], v[192:207]\n"
        "ds_read_b128 v[120:123], v250 offset:2048\n"
        "v_exp_f32 v78, v78\n"
        "v_cvt_pk_bf16_f32 v133, v74, v75\n"
        "v_exp_f32 v79, v79\n"
        "v_cvt_pk_bf16_f32 v134, v76, v77\n"
        "v_cvt_pk_bf16_f32 v135, v78, v79\n"
        "v_exp_f32 v80, v80\n"
        "v_exp_f32 v81, v81\n"
        "s_waitcnt lgkmcnt(1)\n"
        "v_mfma_f32_32x32x16_bf16 v[192:207], v[168:171], v[132:135], v[192:207]\n"
        "ds_read_b128 v[168:171], v250 offset:4608\n"
        "v_exp_f32 v82, v82\n"
        "v_exp_f32 v83, v83\n"
        "v_cvt_pk_bf16_f32 v128, v80, v81\n"
        "v_exp_f32 v84, v84\n"
        "v_exp_f32 v85, v85\n"
        "v_mfma_f32_32x32x16_bf16 v[64:79], v[116:119], v[220:223], 0\n"
        "ds_read_b128 v[116:119], v239 offset:24576\n"
        "v_exp_f32 v86, v86\n"
        "v_cvt_pk_bf16_f32 v129, v82, v83\n"
        "v_exp_f32 v87, v87\n"
        "v_cvt_pk_bf16_f32 v130, v84, v85\n"
        "v_exp_f32 v88, v88\n"
        "v_cvt_pk_bf16_f32 v131, v86, v87\n"
        "v_exp_f32 v89, v89\n"
        "v_mfma_f32_32x32x8_bf16 v[64:79], v[98:99], v[54:55], v[64:79]\n"
        "v_exp_f32 v90, v90\n"
        "v_exp_f32 v91, v91\n"
        "v_cvt_pk_bf16_f32 v132, v88, v89\n"
        "v_exp_f32 v92, v92\n"
        "v_exp_f32 v93, v93\n"
        "v_mfma_f32_32x32x16_bf16 v[192:207], v[124:127], v[128:131], v[192:207]\n"
        "ds_read_b128 v[124:127], v250 offset:0\n"
        "v_exp_f32 v94, v94\n"
        "v_cvt_pk_bf16_f32 v133, v90, v91\n"
        "v_exp_f32 v95, v95\n"
        "v_cvt_pk_bf16_f32 v134, v92, v93\n"
        "v_cvt_pk_bf16_f32 v135, v94, v95\n"
        "v_exp_f32 v64, v64\n"
        "v_exp_f32 v65, v65\n"
        "s_waitcnt lgkmcnt(1)\n"
        "v_mfma_f32_32x32x16_bf16 v[192:207], v[168:171], v[132:135], v[192:207]\n"
        "ds_read_b128 v[168:171], v250 offset:2560\n"
        "v_exp_f32 v66, v66\n"
        "v_exp_f32 v67, v67\n"
        "v_cvt_pk_bf16_f32 v128, v64, v65\n"
        "v_exp_f32 v68, v68\n"
        "v_exp_f32 v69, v69\n"
        "v_mfma_f32_32x32x16_bf16 v[80:95], v[116:119], v[220:223], v[16:31]\n"
        "v_exp_f32 v70, v70\n"
        "v_cvt_pk_bf16_f32 v129, v66, v67\n"
        "v_exp_f32 v71, v71\n"
        "v_cvt_pk_bf16_f32 v130, v68, v69\n"
        "v_exp_f32 v72, v72\n"
        "v_cvt_pk_bf16_f32 v131, v70, v71\n"
        "v_exp_f32 v73, v73\n"
        "v_mfma_f32_32x32x8_bf16 v[80:95], v[96:97], v[54:55], v[80:95]\n"
        "v_exp_f32 v74, v74\n"
        "v_exp_f32 v75, v75\n"
        "v_cvt_pk_bf16_f32 v132, v72, v73\n"
        "v_exp_f32 v76, v76\n"
        "v_exp_f32 v77, v77\n"
        "v_mfma_f32_32x32x16_bf16 v[192:207], v[120:123], v[128:131], v[192:207]\n"
        "v_exp_f32 v78, v78\n"
        "v_cvt_pk_bf16_f32 v133, v74, v75\n"
        "v_exp_f32 v79, v79\n"
        "v_cvt_pk_bf16_f32 v134, v76, v77\n"
        "v_cvt_pk_bf16_f32 v135, v78, v79\n"
        "s_nop 0\n"
        "v_exp_f32 v80, v80\n"
        "v_exp_f32 v81, v81\n"
        "s_waitcnt lgkmcnt(0)\n"
        "v_mfma_f32_32x32x16_bf16 v[192:207], v[168:171], v[132:135], v[192:207]\n"
        "ds_read_b128 v[168:171], v250 offset:512\n"
        "v_exp_f32 v82, v82\n"
        "v_exp_f32 v83, v83\n"
        "v_cvt_pk_bf16_f32 v128, v80, v81\n"
        "v_exp_f32 v84, v84\n"
        "v_exp_f32 v85, v85\n"
        "v_exp_f32 v86, v86\n"
        "v_cvt_pk_bf16_f32 v129, v82, v83\n"
        "v_exp_f32 v87, v87\n"
        "v_cvt_pk_bf16_f32 v130, v84, v85\n"
        "v_exp_f32 v88, v88\n"
        "v_cvt_pk_bf16_f32 v131, v86, v87\n"
        "v_exp_f32 v89, v89\n"
        "v_exp_f32 v90, v90\n"
        "v_exp_f32 v91, v91\n"
        "v_cvt_pk_bf16_f32 v132, v88, v89\n"
        "v_exp_f32 v92, v92\n"
        "v_exp_f32 v93, v93\n"
        "v_mfma_f32_32x32x16_bf16 v[192:207], v[124:127], v[128:131], v[192:207]\n"
        "v_exp_f32 v94, v94\n"
        "v_cvt_pk_bf16_f32 v133, v90, v91\n"
        "v_exp_f32 v95, v95\n"
        "v_cvt_pk_bf16_f32 v134, v92, v93\n"
        "v_cvt_pk_bf16_f32 v135, v94, v95\n"
        "s_waitcnt lgkmcnt(0)\n"
        "s_nop 0\n"
        "v_mfma_f32_32x32x16_bf16 v[192:207], v[168:171], v[132:135], v[192:207]\n"
        "v_mov_b32 v237, 32\n"
        "v_or_b32 v244, 8, v244\n"
        "v_subrev_u32 v67, 1024, v252\n"
        "v_mov_b32 v69, 34\n"
        "v_mov_b32 v66, 33\n"
        "s_mul_i32 s58, s6, 12288\n"
        "s_nop 5\n"
        "v_rcp_f32 v58, v204\n"
        "v_maximum3_f32 v57, |v192|, |v193|, v204\n"
        "v_maximum3_f32 v57, |v194|, |v195|, v57\n"
        "v_maximum3_f32 v57, |v196|, |v197|, v57\n"
        "v_maximum3_f32 v57, |v198|, |v199|, v57\n"
        "v_maximum3_f32 v57, |v202|, |v203|, v57\n"
        "v_maximum3_f32 v57, v58, v57, v57\n"
        "v_cmp_ngt_f32 vcc, s62, v57\n"
        "v_pk_mul_f32 v[192:193], v[192:193], v[58:59] op_sel_hi:[1,0]\n"
        "v_pk_mul_f32 v[194:195], v[194:195], v[58:59] op_sel_hi:[1,0]\n"
        "v_pk_mul_f32 v[196:197], v[196:197], v[58:59] op_sel_hi:[1,0]\n"
        "v_pk_mul_f32 v[198:199], v[198:199], v[58:59] op_sel_hi:[1,0]\n"
        "v_pk_mul_f32 v[202:203], v[202:203], v[58:59] op_sel_hi:[1,0]\n"
        "v_cvt_pk_bf16_f32 v60, v192, v193\n"
        "v_cvt_pk_bf16_f32 v61, v194, v195\n"
        "v_cvt_pk_bf16_f32 v62, v196, v197\n"
        "v_cvt_pk_bf16_f32 v63, v198, v199\n"
        "v_cvt_pk_bf16_f32 v59, v202, v203\n"
        "s_cbranch_vccnz L_SLOW3\n"
        "L_SBACK3:\n"
        "buffer_store_dwordx4 v[60:63], v238, s[28:31], s71 offen sc1\n"
        "buffer_store_dword v59, v243, s[28:31], s75 offen sc1\n"
        "ds_write_b32 v251, v237\n"
        "s_add_u32 s66, s40, s49\n"
        "s_addc_u32 s67, s41, 0\n"
        "s_load_dwordx16 s[68:83], s[66:67], 0 glc\n"
        "s_setprio 0\n"
        "s_or_b64 s[62:63], s[22:23], 15\n"
        "v_subrev_u32 v0, 98304, v242\n"
        "s_waitcnt vmcnt(0)\n"
        "buffer_store_dwordx2 v[244:245], v253, s[40:43], s55 offen sc1\n"
        "s_branch L_S2M\n"
        "L_S2A:\n"
        "s_sub_u32 s56, s56, 1\n"
        "s_cbranch_scc1 L_ABORT\n"
        "s_and_b32 s64, s56, 1023\n"
        "s_cbranch_scc0 Lvl_127\n"
        "s_load_dwordx16 s[68:83], s[66:67], 0 glc\n"
        "L_S2M:\n"
        "s_waitcnt lgkmcnt(0)\n"
        "s_and_b64 s[60:61], s[68:69], s[70:71]\n"
        "s_or_b64 s[64:65], s[68:69], s[70:71]\n"
        "s_and_b64 s[60:61], s[60:61], s[72:73]\n"
        "s_or_b64 s[64:65], s[64:65], s[72:73]\n"
        "s_and_b64 s[60:61], s[60:61], s[74:75]\n"
        "s_or_b64 s[64:65], s[64:65], s[74:75]\n"
        "s_and_b64 s[60:61], s[60:61], s[76:77]\n"
        "s_or_b64 s[64:65], s[64:65], s[76:77]\n"
        "s_and_b64 s[60:61], s[60:61], s[78:79]\n"
        "s_or_b64 s[64:65], s[64:65], s[78:79]\n"
        "s_and_b64 s[60:61], s[60:61], s[80:81]\n"
        "s_or_b64 s[64:65], s[64:65], s[80:81]\n"
        "s_and_b64 s[60:61], s[60:61], s[82:83]\n"
        "s_or_b64 s[64:65], s[64:65], s[82:83]\n"
        "s_or_b64 s[60:61], s[60:61], 11\n"
        "s_cmp_eq_u64 s[60:61], s[62:63]\n"
        "s_cbranch_scc0 L_S2A\n"
        "s_or_b64 s[64:65], s[64:65], 15\n"
        "s_cmp_eq_u64 s[64:65], s[62:63]\n"
        "s_cbranch_scc0 L_S2A\n"
        "ds_write_b32 v251, v66\n"
        "s_load_dwordx16 s[68:83], s[66:67], 0 glc\n"
        "Lw_128:\n"
        "ds_read_b32 v6, v67\n"
        "s_waitcnt lgkmcnt(0)\n"
        "v_cmp_gt_u32 vcc, 32, v6\n"
        "s_cbranch_vccz Lwok_129\n"
        "s_sleep 0\n"
        "s_sub_u32 s56, s56, 1\n"
        "s_cbranch_scc1 L_ABORT\n"
        "s_branch Lw_128\n"
        "Lwok_129:\n"
        "s_movk_i32 s52, 0xff\n"
        "s_add_u32 s57, s58, 73728\n"
        "L_PG:\n"
        "s_mov_b32 s53, 0\n"
        "s_cmp_eq_u64 s[82:83], s[62:63]\n"
        "s_addc_u32 s53, s53, s53\n"
        "s_cmp_eq_u64 s[80:81], s[62:63]\n"
        "s_addc_u32 s53, s53, s53\n"
        "s_cmp_eq_u64 s[78:79], s[62:63]\n"
        "s_addc_u32 s53, s53, s53\n"
        "s_cmp_eq_u64 s[76:77], s[62:63]\n"
        "s_addc_u32 s53, s53, s53\n"
        "s_cmp_eq_u64 s[74:75], s[62:63]\n"
        "s_addc_u32 s53, s53, s53\n"
        "s_cmp_eq_u64 s[72:73], s[62:63]\n"
        "s_addc_u32 s53, s53, s53\n"
        "s_cmp_eq_u64 s[70:71], s[62:63]\n"
        "s_addc_u32 s53, s53, s53\n"
        "s_cmp_eq_u64 s[68:69], s[62:63]\n"
        "s_addc_u32 s53, s53, s53\n"
        "s_load_dwordx16 s[68:83], s[66:67], 0 glc\n"
        "s_and_b32 s53, s53, s52\n"
        "s_cbranch_scc0 L_PGW\n"
        "s_xor_b32 s52, s52, s53\n"
        "L_PGI:\n"
        "s_ff1_i32_b32 s4, s53\n"
        "s_bitset0_b32 s53, s4\n"
        "s_lshl_b32 s4, s4, 10\n"
        "s_add_u32 m0, s57, s4\n"
        "s_add_u32 s4, s4, 49152\n"
        "s_cmp_lg_u32 s53, 0\n"
        "buffer_load_dwordx4 v238, s[28:31], s4 offen sc1 lds\n"
        "s_cbranch_scc1 L_PGI\n"
        "s_cmp_eq_u32 s52, 0\n"
        "s_cbranch_scc1 L_PGD\n"
        "L_PGW:\n"
        "s_sub_u32 s56, s56, 1\n"
        "s_cbranch_scc1 L_ABORT\n"
        "s_and_b32 s64, s56, 1023\n"
        "s_cbranch_scc0 Lvl_130\n"
        "s_waitcnt lgkmcnt(0)\n"
        "s_branch L_PG\n"
        "L_PGD:\n"
        "s_add_u32 m0, s58, 81920\n"
        "s_mov_b32 s4, 68608\n"
        "buffer_load_dwordx4 v238, s[28:31], s4 offen sc1 lds\n"
        "L_PGE:\n"
        "s_waitcnt vmcnt(0)\n"
        "ds_write_b32 v251, v69\n"
        "s_lshl_b32 s4, s20, 2\n"
        "s_or_b32 s4, s4, s6\n"
        "s_cmp_lg_u32 s4, 64\n"
        "s_cbranch_scc1 L_NOGEN\n"
        "v_lshlrev_b32 v1, 13, v0\n"
        "s_or_b64 s[60:61], s[22:23], 1\n"
        "L_GEN:\n"
        "s_mov_b32 s4, 4261904\n"
        "buffer_load_dwordx2 v[8:9], v1, s[40:43], s4 offen sc1\n"
        "s_mov_b32 s4, 4270096\n"
        "buffer_load_dwordx2 v[10:11], v1, s[40:43], s4 offen sc1\n"
        "s_mov_b32 s4, 12650512\n"
        "buffer_load_dwordx2 v[12:13], v1, s[40:43], s4 offen sc1\n"
        "s_mov_b32 s4, 12658704\n"
        "buffer_load_dwordx2 v[14:15], v1, s[40:43], s4 offen sc1\n"
        "s_mov_b64 s[66:67], 0\n"
        "s_waitcnt vmcnt(0)\n"
        "v_cmp_gt_u64 vcc, s[60:61], v[8:9]\n"
        "v_cmp_lt_u64 s[64:65], s[62:63], v[8:9]\n"
        "s_nop 0\n"
        "s_or_b64 s[66:67], s[66:67], vcc\n"
        "s_or_b64 s[66:67], s[66:67], s[64:65]\n"
        "v_cmp_gt_u64 vcc, s[60:61], v[10:11]\n"
        "v_cmp_lt_u64 s[64:65], s[62:63], v[10:11]\n"
        "s_nop 0\n"
        "s_or_b64 s[66:67], s[66:67], vcc\n"
        "s_or_b64 s[66:67], s[66:67], s[64:65]\n"
        "v_cmp_gt_u64 vcc, s[60:61], v[12:13]\n"
        "v_cmp_lt_u64 s[64:65], s[62:63], v[12:13]\n"
        "s_nop 0\n"
        "s_or_b64 s[66:67], s[66:67], vcc\n"
        "s_or_b64 s[66:67], s[66:67], s[64:65]\n"
        "v_cmp_gt_u64 vcc, s[60:61], v[14:15]\n"
        "v_cmp_lt_u64 s[64:65], s[62:63], v[14:15]\n"
        "s_nop 0\n"
        "s_or_b64 s[66:67], s[66:67], vcc\n"
        "s_or_b64 s[66:67], s[66:67], s[64:65]\n"
        "s_cbranch_scc0 L_GENOK\n"
        "s_sub_u32 s56, s56, 1\n"
        "s_cbranch_scc1 L_ABORT\n"
        "s_branch L_GEN\n"
        "L_GENOK:\n"
        "s_add_u32 s24, s16, 0\n"
        "s_addc_u32 s25, s17, 0\n"
        "s_and_b32 s25, s25, 0xffff\n"
        "s_mov_b32 s26, 8\n"
        "s_mov_b32 s27, 0x20000\n"
        "v_mov_b32 v8, s22\n"
        "v_mov_b32 v9, s23\n"
        "buffer_store_dwordx2 v[8:9], v0, s[24:27], 0 offen sc1\n"
        "L_NOGEN:\n"
        "s_endpgm\n"
        "Lpws_114:\n"
        "s_sub_u32 s56, s56, 1\n"
        "s_cbranch_scc1 L_ABORT\n"
        "v_mov_b32 v254, 128\n"
        "global_load_dwordx2 v[254:255], v254, s[16:17] sc1\n"
        "s_waitcnt vmcnt(0)\n"
        "v_readfirstlane_b32 s8, v254\n"
        "v_readfirstlane_b32 s9, v255\n"
        "s_nop 3\n"
        "s_branch Lpw_113\n"
        "Lslow_115:\n"
        "ds_read_b32 v236, v252\n"
        "s_sub_u32 s56, s56, 1\n"
        "s_cbranch_scc1 L_ABORT\n"
        "s_waitcnt lgkmcnt(0)\n"
        "v_cmp_gt_u32 vcc, 4, v236\n"
        "s_cbranch_vccnz Lslow_115\n"
        "s_branch Lback_116\n"
        "Lslow_117:\n"
        "ds_read_b32 v236, v252\n"
        "s_sub_u32 s56, s56, 1\n"
        "s_cbranch_scc1 L_ABORT\n"
        "s_waitcnt lgkmcnt(0)\n"
        "v_cmp_gt_u32 vcc, 6, v236\n"
        "s_cbranch_vccnz Lslow_117\n"
        "s_branch Lback_118\n"
        "Lslow_119:\n"
        "ds_read_b32 v236, v252\n"
        "s_sub_u32 s56, s56, 1\n"
        "s_cbranch_scc1 L_ABORT\n"
        "s_waitcnt lgkmcnt(0)\n"
        "v_cmp_gt_u32 vcc, 7, v236\n"
        "s_cbranch_vccnz Lslow_119\n"
        "s_branch Lback_120\n"
        "Lslow_121:\n"
        "ds_read_b32 v236, v252\n"
        "s_sub_u32 s56, s56, 1\n"
        "s_cbranch_scc1 L_ABORT\n"
        "s_waitcnt lgkmcnt(0)\n"
        "v_cmp_gt_u32 vcc, 8, v236\n"
        "s_cbranch_vccnz Lslow_121\n"
        "s_branch Lback_122\n"
        "Lslow_123:\n"
        "ds_read_b32 v236, v252\n"
        "s_sub_u32 s56, s56, 1\n"
        "s_cbranch_scc1 L_ABORT\n"
        "s_waitcnt lgkmcnt(0)\n"
        "v_cmp_gt_u32 vcc, 9, v236\n"
        "s_cbranch_vccnz Lslow_123\n"
        "s_branch Lback_124\n"
        "Lslow_125:\n"
        "ds_read_b32 v236, v252\n"
        "s_sub_u32 s56, s56, 1\n"
        "s_cbranch_scc1 L_ABORT\n"
        "s_waitcnt lgkmcnt(0)\n"
        "v_cmp_gt_u32 vcc, 10, v236\n"
        "s_cbranch_vccnz Lslow_125\n"
        "s_branch Lback_126\n"
        "Lvl_127:\n"
        "s_call_b64 s[64:65], L_VP68\n"
        "s_branch L_S2M\n"
        "Lvl_130:\n"
        "s_call_b64 s[64:65], L_VP68\n"
        "s_branch L_PG\n"
        "L_END:\n"
        "L_SLOW0:\n"
        "s_nop 15\n"
        "s_nop 15\n"
        "v_mov_b32 v228, v208\n"
        "v_mov_b32 v229, v209\n"
        "v_mov_b32 v230, v210\n"
        "v_mov_b32 v231, v211\n"
        "v_mov_b32 v232, v48\n"
        "v_mov_b32 v233, v49\n"
        "v_add_u32 v172, 0, v238\n"
        "v_mov_b32 v173, v247\n"
        "s_call_b64 s[66:67], L_SLOWFN\n"
        "v_mov_b32 v136, v0\n"
        "v_mov_b32 v137, v1\n"
        "v_mov_b32 v138, v2\n"
        "v_mov_b32 v139, v3\n"
        "v_mov_b32 v140, v4\n"
        "v_mov_b32 v141, v5\n"
        "v_mov_b32 v142, v6\n"
        "v_mov_b32 v143, v7\n"
        "v_mov_b32 v144, v8\n"
        "v_mov_b32 v145, v9\n"
        "v_mov_b32 v146, v10\n"
        "v_mov_b32 v147, v11\n"
        "v_mov_b32 v148, v12\n"
        "v_mov_b32 v149, v13\n"
        "v_mov_b32 v150, v14\n"
        "v_mov_b32 v151, v15\n"
        "v_xor_b32 v0, 0xff000000, v16\n"
        "v_xor_b32 v1, 0xff000000, v17\n"
        "v_xor_b32 v2, 0xff000000, v18\n"
        "v_xor_b32 v3, 0xff000000, v19\n"
        "v_xor_b32 v4, 0xff000000, v20\n"
        "v_xor_b32 v5, 0xff000000, v21\n"
        "v_xor_b32 v6, 0xff000000, v22\n"
        "v_xor_b32 v7, 0xff000000, v23\n"
        "v_xor_b32 v8, 0xff000000, v24\n"
        "v_xor_b32 v9, 0xff000000, v25\n"
        "v_xor_b32 v10, 0xff000000, v26\n"
        "v_xor_b32 v11, 0xff000000, v27\n"
        "v_xor_b32 v12, 0xff000000, v28\n"
        "v_xor_b32 v13, 0xff000000, v29\n"
        "v_xor_b32 v14, 0xff000000, v30\n"
        "v_xor_b32 v15, 0xff000000, v31\n"
        "v_rcp_f32 v58, v148\n"
        "s_nop 0\n"
        "v_pk_mul_f32 v[136:137], v[136:137], v[58:59] op_sel_hi:[1,0]\n"
        "v_pk_mul_f32 v[138:139], v[138:139], v[58:59] op_sel_hi:[1,0]\n"
        "v_pk_mul_f32 v[140:141], v[140:141], v[58:59] op_sel_hi:[1,0]\n"
        "v_pk_mul_f32 v[142:143], v[142:143], v[58:59] op_sel_hi:[1,0]\n"
        "v_pk_mul_f32 v[144:145], v[144:145], v[58:59] op_sel_hi:[1,0]\n"
        "v_cvt_pk_bf16_f32 v60, v136, v137\n"
        "v_cvt_pk_bf16_f32 v61, v138, v139\n"
        "v_cvt_pk_bf16_f32 v62, v140, v141\n"
        "v_cvt_pk_bf16_f32 v63, v142, v143\n"
        "v_cvt_pk_bf16_f32 v59, v144, v145\n"
        "s_nop 3\n"
        "s_branch L_SBACK0\n"
        "L_SLOW1:\n"
        "s_nop 15\n"
        "s_nop 15\n"
        "v_mov_b32 v228, v212\n"
        "v_mov_b32 v229, v213\n"
        "v_mov_b32 v230, v214\n"
        "v_mov_b32 v231, v215\n"
        "v_mov_b32 v232, v50\n"
        "v_mov_b32 v233, v51\n"
        "v_add_u32 v172, 24576, v238\n"
        "v_mov_b32 v173, v248\n"
        "s_call_b64 s[66:67], L_SLOWFN\n"
        "v_mov_b32 v152, v0\n"
        "v_mov_b32 v153, v1\n"
        "v_mov_b32 v154, v2\n"
        "v_mov_b32 v155, v3\n"
        "v_mov_b32 v156, v4\n"
        "v_mov_b32 v157, v5\n"
        "v_mov_b32 v158, v6\n"
        "v_mov_b32 v159, v7\n"
        "v_mov_b32 v160, v8\n"
        "v_mov_b32 v161, v9\n"
        "v_mov_b32 v162, v10\n"
        "v_mov_b32 v163, v11\n"
        "v_mov_b32 v164, v12\n"
        "v_mov_b32 v165, v13\n"
        "v_mov_b32 v166, v14\n"
        "v_mov_b32 v167, v15\n"
        "v_xor_b32 v0, 0xff000000, v16\n"
        "v_xor_b32 v1, 0xff000000, v17\n"
        "v_xor_b32 v2, 0xff000000, v18\n"
        "v_xor_b32 v3, 0xff000000, v19\n"
        "v_xor_b32 v4, 0xff000000, v20\n"
        "v_xor_b32 v5, 0xff000000, v21\n"
        "v_xor_b32 v6, 0xff000000, v22\n"
        "v_xor_b32 v7, 0xff000000, v23\n"
        "v_xor_b32 v8, 0xff000000, v24\n"
        "v_xor_b32 v9, 0xff000000, v25\n"
        "v_xor_b32 v10, 0xff000000, v26\n"
        "v_xor_b32 v11, 0xff000000, v27\n"
        "v_xor_b32 v12, 0xff000000, v28\n"
        "v_xor_b32 v13, 0xff000000, v29\n"
        "v_xor_b32 v14, 0xff000000, v30\n"
        "v_xor_b32 v15, 0xff000000, v31\n"
        "v_rcp_f32 v58, v164\n"
        "s_nop 0\n"
        "v_pk_mul_f32 v[152:153], v[152:153], v[58:59] op_sel_hi:[1,0]\n"
        "v_pk_mul_f32 v[154:155], v[154:155], v[58:59] op_sel_hi:[1,0]\n"
        "v_pk_mul_f32 v[156:157], v[156:157], v[58:59] op_sel_hi:[1,0]\n"
        "v_pk_mul_f32 v[158:159], v[158:159], v[58:59] op_sel_hi:[1,0]\n"
        "v_pk_mul_f32 v[162:163], v[162:163], v[58:59] op_sel_hi:[1,0]\n"
        "v_cvt_pk_bf16_f32 v60, v152, v153\n"
        "v_cvt_pk_bf16_f32 v61, v154, v155\n"
        "v_cvt_pk_bf16_f32 v62, v156, v157\n"
        "v_cvt_pk_bf16_f32 v63, v158, v159\n"
        "v_cvt_pk_bf16_f32 v59, v162, v163\n"
        "s_nop 3\n"
        "s_branch L_SBACK1\n"
        "L_SLOW2:\n"
        "s_nop 15\n"
        "s_nop 15\n"
        "v_mov_b32 v228, v216\n"
        "v_mov_b32 v229, v217\n"
        "v_mov_b32 v230, v218\n"
        "v_mov_b32 v231, v219\n"
        "v_mov_b32 v232, v52\n"
        "v_mov_b32 v233, v53\n"
        "v_add_u32 v172, 0, v239\n"
        "v_mov_b32 v173, v249\n"
        "s_call_b64 s[66:67], L_SLOWFN\n"
        "v_mov_b32 v32, v0\n"
        "v_mov_b32 v33, v1\n"
        "v_mov_b32 v34, v2\n"
        "v_mov_b32 v35, v3\n"
        "v_mov_b32 v36, v4\n"
        "v_mov_b32 v37, v5\n"
        "v_mov_b32 v38, v6\n"
        "v_mov_b32 v39, v7\n"
        "v_mov_b32 v40, v8\n"
        "v_mov_b32 v41, v9\n"
        "v_mov_b32 v42, v10\n"
        "v_mov_b32 v43, v11\n"
        "v_mov_b32 v44, v12\n"
        "v_mov_b32 v45, v13\n"
        "v_mov_b32 v46, v14\n"
        "v_mov_b32 v47, v15\n"
        "v_xor_b32 v0, 0xff000000, v16\n"
        "v_xor_b32 v1, 0xff000000, v17\n"
        "v_xor_b32 v2, 0xff000000, v18\n"
        "v_xor_b32 v3, 0xff000000, v19\n"
        "v_xor_b32 v4, 0xff000000, v20\n"
        "v_xor_b32 v5, 0xff000000, v21\n"
        "v_xor_b32 v6, 0xff000000, v22\n"
        "v_xor_b32 v7, 0xff000000, v23\n"
        "v_xor_b32 v8, 0xff000000, v24\n"
        "v_xor_b32 v9, 0xff000000, v25\n"
        "v_xor_b32 v10, 0xff000000, v26\n"
        "v_xor_b32 v11, 0xff000000, v27\n"
        "v_xor_b32 v12, 0xff000000, v28\n"
        "v_xor_b32 v13, 0xff000000, v29\n"
        "v_xor_b32 v14, 0xff000000, v30\n"
        "v_xor_b32 v15, 0xff000000, v31\n"
        "v_rcp_f32 v58, v44\n"
        "s_nop 0\n"
        "v_pk_mul_f32 v[32:33], v[32:33], v[58:59] op_sel_hi:[1,0]\n"
        "v_pk_mul_f32 v[34:35], v[34:35], v[58:59] op_sel_hi:[1,0]\n"
        "v_pk_mul_f32 v[36:37], v[36:37], v[58:59] op_sel_hi:[1,0]\n"
        "v_pk_mul_f32 v[38:39], v[38:39], v[58:59] op_sel_hi:[1,0]\n"
        "v_pk_mul_f32 v[40:41], v[40:41], v[58:59] op_sel_hi:[1,0]\n"
        "v_cvt_pk_bf16_f32 v60, v32, v33\n"
        "v_cvt_pk_bf16_f32 v61, v34, v35\n"
        "v_cvt_pk_bf16_f32 v62, v36, v37\n"
        "v_cvt_pk_bf16_f32 v63, v38, v39\n"
        "v_cvt_pk_bf16_f32 v59, v40, v41\n"
        "s_nop 3\n"
        "s_branch L_SBACK2\n"
        "L_SLOW3:\n"
        "s_nop 15\n"
        "s_nop 15\n"
        "v_mov_b32 v228, v220\n"
        "v_mov_b32 v229, v221\n"
        "v_mov_b32 v230, v222\n"
        "v_mov_b32 v231, v223\n"
        "v_mov_b32 v232, v54\n"
        "v_mov_b32 v233, v55\n"
        "v_add_u32 v172, 24576, v239\n"
        "v_mov_b32 v173, v250\n"
        "s_call_b64 s[66:67], L_SLOWFN\n"
        "v_mov_b32 v192, v0\n"
        "v_mov_b32 v193, v1\n"
        "v_mov_b32 v194, v2\n"
        "v_mov_b32 v195, v3\n"
        "v_mov_b32 v196, v4\n"
        "v_mov_b32 v197, v5\n"
        "v_mov_b32 v198, v6\n"
        "v_mov_b32 v199, v7\n"
        "v_mov_b32 v200, v8\n"
        "v_mov_b32 v201, v9\n"
        "v_mov_b32 v202, v10\n"
        "v_mov_b32 v203, v11\n"
        "v_mov_b32 v204, v12\n"
        "v_mov_b32 v205, v13\n"
        "v_mov_b32 v206, v14\n"
        "v_mov_b32 v207, v15\n"
        "v_xor_b32 v0, 0xff000000, v16\n"
        "v_xor_b32 v1, 0xff000000, v17\n"
        "v_xor_b32 v2, 0xff000000, v18\n"
        "v_xor_b32 v3, 0xff000000, v19\n"
        "v_xor_b32 v4, 0xff000000, v20\n"
        "v_xor_b32 v5, 0xff000000, v21\n"
        "v_xor_b32 v6, 0xff000000, v22\n"
        "v_xor_b32 v7, 0xff000000, v23\n"
        "v_xor_b32 v8, 0xff000000, v24\n"
        "v_xor_b32 v9, 0xff000000, v25\n"
        "v_xor_b32 v10, 0xff000000, v26\n"
        "v_xor_b32 v11, 0xff000000, v27\n"
        "v_xor_b32 v12, 0xff000000, v28\n"
        "v_xor_b32 v13, 0xff000000, v29\n"
        "v_xor_b32 v14, 0xff000000, v30\n"
        "v_xor_b32 v15, 0xff000000, v31\n"
        "v_rcp_f32 v58, v204\n"
        "s_nop 0\n"
        "v_pk_mul_f32 v[192:193], v[192:193], v[58:59] op_sel_hi:[1,0]\n"
        "v_pk_mul_f32 v[194:195], v[194:195], v[58:59] op_sel_hi:[1,0]\n"
        "v_pk_mul_f32 v[196:197], v[196:197], v[58:59] op_sel_hi:[1,0]\n"
        "v_pk_mul_f32 v[198:199], v[198:199], v[58:59] op_sel_hi:[1,0]\n"
        "v_pk_mul_f32 v[202:203], v[202:203], v[58:59] op_sel_hi:[1,0]\n"
        "v_cvt_pk_bf16_f32 v60, v192, v193\n"
        "v_cvt_pk_bf16_f32 v61, v194, v195\n"
        "v_cvt_pk_bf16_f32 v62, v196, v197\n"
        "v_cvt_pk_bf16_f32 v63, v198, v199\n"
        "v_cvt_pk_bf16_f32 v59, v202, v203\n"
        "s_nop 3\n"
        "s_branch L_SBACK3\n"
        "L_SLOWFN:\n"
        "v_add_u32 v174, 98304, v242\n"
        "v_lshrrev_b32 v174, 1, v174\n"
        "v_mov_b32 v175, 0xff800000\n"
        "s_mov_b32 s52, 8\n"
        "L_SA:\n"
        "s_lshl_b32 s4, s52, 11\n"
        "v_add_u32 v114, s4, v172\n"
        "ds_read_b128 v[224:227], v114\n"
        "s_add_u32 s7, s52, s6\n"
        "s_and_b32 s57, s7, 1\n"
        "s_lshl_b32 s57, s57, 9\n"
        "s_and_b32 s7, s7, -2\n"
        "s_lshl_b32 s7, s7, 11\n"
        "s_add_u32 s7, s7, s57\n"
        "v_add_u32 v115, s7, v174\n"
        "ds_read_b64 v[234:235], v115\n"
        "s_waitcnt lgkmcnt(0)\n"
        "s_cmp_eq_u32 s52, 8\n"
        "s_cbranch_scc1 L_SDA\n"
        "s_cmp_eq_u32 s52, 0\n"
        "s_cbranch_scc1 L_SFA\n"
        "v_mfma_f32_32x32x16_bf16 v[176:191], v[224:227], v[228:231], 0\n"
        "s_branch L_SJA\n"
        "L_SDA:\n"
        "v_xor_b32 v176, 0xff000000, v16\n"
        "v_xor_b32 v177, 0xff000000, v17\n"
        "v_xor_b32 v178, 0xff000000, v18\n"
        "v_xor_b32 v179, 0xff000000, v19\n"
        "v_xor_b32 v180, 0xff000000, v20\n"
        "v_xor_b32 v181, 0xff000000, v21\n"
        "v_xor_b32 v182, 0xff000000, v22\n"
        "v_xor_b32 v183, 0xff000000, v23\n"
        "v_xor_b32 v184, 0xff000000, v24\n"
        "v_xor_b32 v185, 0xff000000, v25\n"
        "v_xor_b32 v186, 0xff000000, v26\n"
        "v_xor_b32 v187, 0xff000000, v27\n"
        "v_xor_b32 v188, 0xff000000, v28\n"
        "v_xor_b32 v189, 0xff000000, v29\n"
        "v_xor_b32 v190, 0xff000000, v30\n"
        "v_xor_b32 v191, 0xff000000, v31\n"
        "s_nop 1\n"
        "v_mfma_f32_32x32x16_bf16 v[176:191], v[224:227], v[228:231], v[176:191]\n"
        "s_branch L_SJA\n"
        "L_SFA:\n"
        "v_mfma_f32_32x32x16_bf16 v[176:191], v[224:227], v[228:231], v[16:31]\n"
        "L_SJA:\n"
        "v_mfma_f32_32x32x8_bf16 v[176:191], v[234:235], v[232:233], v[176:191]\n"
        "s_nop 15\n"
        "v_max3_f32 v175, v175, v176, v177\n"
        "v_max3_f32 v175, v175, v178, v179\n"
        "v_max3_f32 v175, v175, v180, v181\n"
        "v_max3_f32 v175, v175, v182, v183\n"
        "v_max3_f32 v175, v175, v184, v185\n"
        "v_max3_f32 v175, v175, v186, v187\n"
        "v_max3_f32 v175, v175, v188, v189\n"
        "v_max3_f32 v175, v175, v190, v191\n"
        "s_sub_i32 s52, s52, 1\n"
        "s_cmp_ge_i32 s52, s53\n"
        "s_cbranch_scc1 L_SA\n"
        "v_mov_b32 v114, v175\n"
        "s_nop 1\n"
        "v_permlane32_swap_b32 v175, v114\n"
        "v_max_f32 v175, v175, v114\n"
        "s_mov_b32 s52, 8\n"
        "v_mov_b32 v0, 0\n"
        "v_mov_b32 v1, 0\n"
        "v_mov_b32 v2, 0\n"
        "v_mov_b32 v3, 0\n"
        "v_mov_b32 v4, 0\n"
        "v_mov_b32 v5, 0\n"
        "v_mov_b32 v6, 0\n"
        "v_mov_b32 v7, 0\n"
        "v_mov_b32 v8, 0\n"
        "v_mov_b32 v9, 0\n"
        "v_mov_b32 v10, 0\n"
        "v_mov_b32 v11, 0\n"
        "v_mov_b32 v12, 0\n"
        "v_mov_b32 v13, 0\n"
        "v_mov_b32 v14, 0\n"
        "v_mov_b32 v15, 0\n"
        "L_SB:\n"
        "s_lshl_b32 s4, s52, 11\n"
        "v_add_u32 v114, s4, v172\n"
        "ds_read_b128 v[224:227], v114\n"
        "s_add_u32 s7, s52, s6\n"
        "s_and_b32 s57, s7, 1\n"
        "s_lshl_b32 s57, s57, 9\n"
        "s_and_b32 s7, s7, -2\n"
        "s_lshl_b32 s7, s7, 11\n"
        "s_add_u32 s7, s7, s57\n"
        "v_add_u32 v115, s7, v174\n"
        "ds_read_b64 v[234:235], v115\n"
        "s_waitcnt lgkmcnt(0)\n"
        "s_cmp_eq_u32 s52, 8\n"
        "s_cbranch_scc1 L_SDB\n"
        "s_cmp_eq_u32 s52, 0\n"
        "s_cbranch_scc1 L_SFB\n"
        "v_mfma_f32_32x32x16_bf16 v[176:191], v[224:227], v[228:231], 0\n"
        "s_branch L_SJB\n"
        "L_SDB:\n"
        "v_xor_b32 v176, 0xff000000, v16\n"
        "v_xor_b32 v177, 0xff000000, v17\n"
        "v_xor_b32 v178, 0xff000000, v18\n"
        "v_xor_b32 v179, 0xff000000, v19\n"
        "v_xor_b32 v180, 0xff000000, v20\n"
        "v_xor_b32 v181, 0xff000000, v21\n"
        "v_xor_b32 v182, 0xff000000, v22\n"
        "v_xor_b32 v183, 0xff000000, v23\n"
        "v_xor_b32 v184, 0xff000000, v24\n"
        "v_xor_b32 v185, 0xff000000, v25\n"
        "v_xor_b32 v186, 0xff000000, v26\n"
        "v_xor_b32 v187, 0xff000000, v27\n"
        "v_xor_b32 v188, 0xff000000, v28\n"
        "v_xor_b32 v189, 0xff000000, v29\n"
        "v_xor_b32 v190, 0xff000000, v30\n"
        "v_xor_b32 v191, 0xff000000, v31\n"
        "s_nop 1\n"
        "v_mfma_f32_32x32x16_bf16 v[176:191], v[224:227], v[228:231], v[176:191]\n"
        "s_branch L_SJB\n"
        "L_SFB:\n"
        "v_mfma_f32_32x32x16_bf16 v[176:191], v[224:227], v[228:231], v[16:31]\n"
        "L_SJB:\n"
        "v_mfma_f32_32x32x8_bf16 v[176:191], v[234:235], v[232:233], v[176:191]\n"
        "s_nop 15\n"
        "v_sub_f32 v176, v176, v175\n"
        "v_sub_f32 v177, v177, v175\n"
        "v_sub_f32 v178, v178, v175\n"
        "v_sub_f32 v179, v179, v175\n"
        "v_sub_f32 v180, v180, v175\n"
        "v_sub_f32 v181, v181, v175\n"
        "v_sub_f32 v182, v182, v175\n"
        "v_sub_f32 v183, v183, v175\n"
        "v_sub_f32 v184, v184, v175\n"
        "v_sub_f32 v185, v185, v175\n"
        "v_sub_f32 v186, v186, v175\n"
        "v_sub_f32 v187, v187, v175\n"
        "v_sub_f32 v188, v188, v175\n"
        "v_sub_f32 v189, v189, v175\n"
        "v_sub_f32 v190, v190, v175\n"
        "v_sub_f32 v191, v191, v175\n"
        "v_exp_f32 v176, v176\n"
        "v_exp_f32 v177, v177\n"
        "v_exp_f32 v178, v178\n"
        "v_exp_f32 v179, v179\n"
        "v_exp_f32 v180, v180\n"
        "v_exp_f32 v181, v181\n"
        "v_exp_f32 v182, v182\n"
        "v_exp_f32 v183, v183\n"
        "v_exp_f32 v184, v184\n"
        "v_exp_f32 v185, v185\n"
        "v_exp_f32 v186, v186\n"
        "v_exp_f32 v187, v187\n"
        "v_exp_f32 v188, v188\n"
        "v_exp_f32 v189, v189\n"
        "v_exp_f32 v190, v190\n"
        "v_exp_f32 v191, v191\n"
        "v_cvt_pk_bf16_f32 v176, v176, v177\n"
        "v_cvt_pk_bf16_f32 v177, v178, v179\n"
        "v_cvt_pk_bf16_f32 v178, v180, v181\n"
        "v_cvt_pk_bf16_f32 v179, v182, v183\n"
        "v_cvt_pk_bf16_f32 v180, v184, v185\n"
        "v_cvt_pk_bf16_f32 v181, v186, v187\n"
        "v_cvt_pk_bf16_f32 v182, v188, v189\n"
        "v_cvt_pk_bf16_f32 v183, v190, v191\n"
        "v_add_u32 v114, s4, v173\n"
        "ds_read_b128 v[184:187], v114\n"
        "ds_read_b128 v[188:191], v114 offset:512\n"
        "s_waitcnt lgkmcnt(0)\n"
        "v_mfma_f32_32x32x16_bf16 v[0:15], v[184:187], v[176:179], v[0:15]\n"
        "v_mfma_f32_32x32x16_bf16 v[0:15], v[188:191], v[180:183], v[0:15]\n"
        "s_nop 15\n"
        "s_sub_i32 s52, s52, 1\n"
        "s_cmp_ge_i32 s52, s53\n"
        "s_cbranch_scc1 L_SB\n"
        "s_setpc_b64 s[66:67]\n"
        "L_VP8:\n"
        "v_mbcnt_lo_u32_b32 v20, -1, 0\n"
        "v_mbcnt_hi_u32_b32 v20, -1, v20\n"
        "v_min_u32 v20, 1, v20\n"
        "v_lshlrev_b32 v20, 3, v20\n"
        "s_waitcnt lgkmcnt(0)\n"
        "global_load_dwordx2 v[20:21], v20, s[66:67] sc1\n"
        "s_waitcnt vmcnt(0)\n"
        "v_readlane_b32 s8, v20, 0\n"
        "v_readlane_b32 s9, v21, 0\n"
        "v_readlane_b32 s10, v20, 1\n"
        "v_readlane_b32 s11, v21, 1\n"
        "s_nop 3\n"
        "s_setpc_b64 s[64:65]\n"
        "L_VP24:\n"
        "v_mbcnt_lo_u32_b32 v20, -1, 0\n"
        "v_mbcnt_hi_u32_b32 v20, -1, v20\n"
        "v_min_u32 v20, 7, v20\n"
        "v_lshlrev_b32 v20, 3, v20\n"
        "s_waitcnt lgkmcnt(0)\n"
        "global_load_dwordx2 v[20:21], v20, s[66:67] sc1\n"
        "s_waitcnt vmcnt(0)\n"
        "v_readlane_b32 s24, v20, 0\n"
        "v_readlane_b32 s25, v21, 0\n"
        "v_readlane_b32 s26, v20, 1\n"
        "v_readlane_b32 s27, v21, 1\n"
        "v_readlane_b32 s28, v20, 2\n"
        "v_readlane_b32 s29, v21, 2\n"
        "v_readlane_b32 s30, v20, 3\n"
        "v_readlane_b32 s31, v21, 3\n"
        "v_readlane_b32 s32, v20, 4\n"
        "v_readlane_b32 s33, v21, 4\n"
        "v_readlane_b32 s34, v20, 5\n"
        "v_readlane_b32 s35, v21, 5\n"
        "v_readlane_b32 s36, v20, 6\n"
        "v_readlane_b32 s37, v21, 6\n"
        "v_readlane_b32 s38, v20, 7\n"
        "v_readlane_b32 s39, v21, 7\n"
        "s_nop 3\n"
        "s_setpc_b64 s[64:65]\n"
        "L_VP68:\n"
        "v_mbcnt_lo_u32_b32 v6, -1, 0\n"
        "v_mbcnt_hi_u32_b32 v6, -1, v6\n"
        "v_min_u32 v6, 7, v6\n"
        "v_lshlrev_b32 v6, 3, v6\n"
        "s_waitcnt lgkmcnt(0)\n"
        "global_load_dwordx2 v[6:7], v6, s[66:67] sc1\n"
        "s_waitcnt vmcnt(0)\n"
        "v_readlane_b32 s68, v6, 0\n"
        "v_readlane_b32 s69, v7, 0\n"
        "v_readlane_b32 s70, v6, 1\n"
        "v_readlane_b32 s71, v7, 1\n"
        "v_readlane_b32 s72, v6, 2\n"
        "v_readlane_b32 s73, v7, 2\n"
        "v_readlane_b32 s74, v6, 3\n"
        "v_readlane_b32 s75, v7, 3\n"
        "v_readlane_b32 s76, v6, 4\n"
        "v_readlane_b32 s77, v7, 4\n"
        "v_readlane_b32 s78, v6, 5\n"
        "v_readlane_b32 s79, v7, 5\n"
        "v_readlane_b32 s80, v6, 6\n"
        "v_readlane_b32 s81, v7, 6\n"
        "v_readlane_b32 s82, v6, 7\n"
        "v_readlane_b32 s83, v7, 7\n"
        "s_nop 3\n"
        "s_setpc_b64 s[64:65]\n"
        :
        : "{s[0:1]}"(__builtin_amdgcn_kernarg_segment_ptr()), "{s2}"(__builtin_amdgcn_workgroup_id_x()),
          "{v0}"(__builtin_amdgcn_workitem_id_x()), "i"((__attribute__((address_space(3))) char*)lds)
        : "memory", "vcc", "scc", "v1", "v2", "v3", "v4", "v5", "v6", "v7", "v8", "v9", "v10", "v11", "v12", "v13", "v14", "v15", "v16", "v17", "v18", "v19", "v20", "v21", "v22", "v23", "v24", "v25", "v26", "v27", "v28", "v29", "v30", "v31", "v32", "v33", "v34", "v35", "v36", "v37", "v38", "v39", "v40", "v41", "v42", "v43", "v44", "v45", "v46", "v47", "v48", "v49", "v50", "v51", "v52", "v53", "v54", "v55", "v56", "v57", "v58", "v59", "v60", "v61", "v62", "v63", "v64", "v65", "v66", "v67", "v68", "v69", "v70", "v71", "v72", "v73", "v74", "v75", "v76", "v77", "v78", "v79", "v80", "v81", "v82", "v83", "v84", "v85", "v86", "v87", "v88", "v89", "v90", "v91", "v92", "v93", "v94", "v95", "v96", "v97", "v98", "v99", "v100", "v101", "v102", "v103", "v104", "v105", "v106", "v107", "v108", "v109", "v110", "v111", "v112", "v113", "v114", "v115", "v116", "v117", "v118", "v119", "v120", "v121", "v122", "v123", "v124", "v125", "v126", "v127", "v128", "v129", "v130", "v131", "v132", "v133", "v134", "v135", "v136", "v137", "v138", "v139", "v140", "v141", "v142", "v143", "v144", "v145", "v146", "v147", "v148", "v149", "v150", "v151", "v152", "v153", "v154", "v155", "v156", "v157", "v158", "v159", "v160", "v161", "v162", "v163", "v164", "v165", "v166", "v167", "v168", "v169", "v170", "v171", "v172", "v173", "v174", "v175", "v176", "v177", "v178", "v179", "v180", "v181", "v182", "v183", "v184", "v185", "v186", "v187", "v188", "v189", "v190", "v191", "v192", "v193", "v194", "v195", "v196", "v197", "v198", "v199", "v200", "v201", "v202", "v203", "v204", "v205", "v206", "v207", "v208", "v209", "v210", "v211", "v212", "v213", "v214", "v215", "v216", "v217", "v218", "v219", "v220", "v221", "v222", "v223", "v224", "v225", "v226", "v227", "v228", "v229", "v230", "v231", "v232", "v233", "v234", "v235", "v236", "v237", "v238", "v239", "v240", "v241", "v242", "v243", "v244", "v245", "v246", "v247", "v248", "v249", "v250", "v251", "v252", "v253", "v254", "v255", "s3", "s4", "s5", "s6", "s7", "s8", "s9", "s10", "s11", "s12", "s13", "s14", "s15", "s16", "s17", "s18", "s19", "s20", "s21", "s22", "s23", "s24", "s25", "s26", "s27", "s28", "s29", "s30", "s31", "s33", "s34", "s35", "s36", "s37", "s38", "s39", "s40", "s41", "s42", "s43", "s44", "s45", "s46", "s47", "s48", "s49", "s50", "s51", "s52", "s53", "s54", "s55", "s56", "s57", "s58", "s59", "s60", "s61", "s62", "s63", "s64", "s65", "s66", "s67", "s68", "s69", "s70", "s71", "s72", "s73", "s74", "s75", "s76", "s77", "s78", "s79", "s80", "s81", "s82", "s83");
}

extern "C" void kernel_launch(void* const* d_in, const int* in_sizes, int n_in, void* d_out, int out_size, void* d_ws,
                              size_t ws_size, hipStream_t stream) {
    fused_attn_kernel<<<256, 512, 0, stream>>>(d_in[0], d_in[1], d_in[2], d_out, d_in[3], d_in[4], d_in[5],
                                               (char*)d_out + 1152, d_ws);
}
```
